```python
import torch

def _softmax(x, axis=-1):
    return torch.softmax(x, dim=axis)

def _gelu(x):
    return torch.nn.functional.gelu(x, approximate='tanh')

def _silu(x):
    return torch.nn.functional.silu(x)

def _sigmoid(x):
    return torch.sigmoid(x)

def _layernorm(x, axis=-1, eps=1e-05):
    m = x.mean(dim=axis, keepdim=True)
    v = x.var(dim=axis, keepdim=True, unbiased=False)
    return (x - m) / torch.sqrt(v + eps)

def _scatter_add(tgt, idx, src, axis=0):
    return tgt.clone().index_add_(axis, idx, src)

def _segment_sum(src, seg, n):
    out = torch.zeros((n,) + src.shape[1:], dtype=src.dtype)
    return out.index_add_(0, seg, src)

def _pad(x, pw):
    flat = tuple((v for pair in reversed(pw) for v in pair))
    return torch.nn.functional.pad(x, flat)
kernel_name = 'adaLN_cond__spectral_fnet__dense_activation_relu__prompt'

def setup_inputs():

    _w = lambda a: (lambda t: t.to(torch.bfloat16) if t.is_floating_point() else t)(torch.as_tensor(a))

    def _A_setup():
        B, S, D = (1, 2048, 512)
        ns = torch.arange(S)[:, None] * torch.arange(S)[None, :]
        nd = torch.arange(D)[:, None] * torch.arange(D)[None, :]
        Ws_re = torch.cos(-2.0 * torch.pi * ns / S).to(float)
        Ws_im = torch.sin(-2.0 * torch.pi * ns / S).to(float)
        Wd_re = torch.cos(-2.0 * torch.pi * nd / D).to(float)
        Wd_im = torch.sin(-2.0 * torch.pi * nd / D).to(float)
        return {'x': _w((lambda *s: torch.randn(*s, dtype=torch.bfloat16) * 0.1)(B, S, D)), 'dft_seq_re': _w(Ws_re), 'dft_seq_im': _w(Ws_im), 'dft_hid_re': _w(Wd_re), 'dft_hid_im': _w(Wd_im)}
    _A_in = _A_setup()

    def _B_setup():
        B, S, D, Dff = (1, 2048, 512, 1792)
        return {'x': _w((lambda *s: torch.randn(*s, dtype=torch.bfloat16) * 0.1)(B, S, D)), 'w1': _w((lambda *s: torch.randn(*s, dtype=torch.bfloat16) * 3.2)(D, Dff)), 'w2': _w((lambda *s: torch.randn(*s, dtype=torch.bfloat16) * 3.2)(Dff, D))}
    _B_in = _B_setup()
    return {'A_x': _A_in['x'], 'A_dft_seq_re': _A_in['dft_seq_re'], 'A_dft_seq_im': _A_in['dft_seq_im'], 'A_dft_hid_re': _A_in['dft_hid_re'], 'A_dft_hid_im': _A_in['dft_hid_im'], 'B_x': _B_in['x'], 'B_w1': _B_in['w1'], 'B_w2': _B_in['w2'], 'c': _w((lambda *s: torch.randn(*s, dtype=torch.bfloat16) * 0.4)(1, 512) * 8.0), 'w_mod': _w((lambda *s: torch.randn(*s, dtype=torch.bfloat16) * 0.4)(512, 6 * 512) * 512 ** (-0.5) * 8.0)}

def reference(A_x, A_dft_seq_re, A_dft_seq_im, A_dft_hid_re, A_dft_hid_im, B_x, B_w1, B_w2, c, w_mod):

    def _A_ref(x, dft_seq_re, dft_seq_im, dft_hid_re, dft_hid_im):
        a = torch.einsum('bsd,ed->bse', x, dft_hid_re)
        b = torch.einsum('bsd,ed->bse', x, dft_hid_im)
        y = torch.einsum('ts,bse->bte', dft_seq_re, a) - torch.einsum('ts,bse->bte', dft_seq_im, b)
        return y

    def _B_ref(x, w1, w2):
        h = torch.einsum('bsd,df->bsf', x, w1)
        zero = torch.zeros_like(h)
        h = torch.maximum(h, zero)
        return torch.einsum('bsf,fd->bsd', h, w2)
    x = A_x
    mod = (_silu(c) @ w_mod).reshape(c.shape[0], 6, 1, -1)
    sa, ca, ga, sb, cb, gb = (mod[:, 0].clone(), mod[:, 1].clone(), mod[:, 2].clone(), mod[:, 3].clone(), mod[:, 4].clone(), mod[:, 5].clone())
    h = _layernorm(x, axis=-1) * (1 + ca) + sa
    x = x + ga * _A_ref(h, A_dft_seq_re, A_dft_seq_im, A_dft_hid_re, A_dft_hid_im)
    h = _layernorm(x, axis=-1) * (1 + cb) + sb
    x = x + gb * _B_ref(h, B_w1, B_w2)
    return x
```

```cpp
#include <hip/hip_runtime.h>
#include <hip/hip_bf16.h>
#include <stdint.h>

typedef short bf16x8 __attribute__((ext_vector_type(8)));
typedef float f32x4 __attribute__((ext_vector_type(4)));
typedef unsigned short u16;

constexpr int S = 2048, D = 512, F = 1792;

__device__ __forceinline__ float bf2f(u16 b) { return __builtin_bit_cast(float, (unsigned)b << 16); }
__device__ __forceinline__ u16 f2bf(float f) { __hip_bfloat16 h = __float2bfloat16(f); return *reinterpret_cast<u16*>(&h); }

__global__ void k_mod(const u16* c, const u16* wm, float* mod) {
  int col = blockIdx.x * 64 + (threadIdx.x & 63);
  int q = threadIdx.x >> 6;
  float acc = 0.f;
  for (int k = q * 128; k < q * 128 + 128; ++k) {
    float v = bf2f(c[k]);
    float s = v / (1.f + __expf(-v));
    acc += s * bf2f(wm[k * 3072 + col]);
  }
  __shared__ float red[256];
  red[threadIdx.x] = acc;
  __syncthreads();
  if (q == 0) mod[col] = red[threadIdx.x] + red[threadIdx.x + 64] + red[threadIdx.x + 128] + red[threadIdx.x + 192];
}

template <typename T> __device__ float ld(const T* p);
template <> __device__ float ld<u16>(const u16* p) { return bf2f(*p); }
template <> __device__ float ld<float>(const float* p) { return *p; }

template <typename T>
__global__ void k_ln(const T* x, const float* shift, const float* scale, u16* h) {
  int row = blockIdx.x * 4 + (threadIdx.x >> 6), lane = threadIdx.x & 63;
  float v[8];
  float s = 0.f;
  for (int j = 0; j < 8; ++j) { v[j] = ld<T>(x + row * D + lane * 8 + j); s += v[j]; }
  for (int o = 32; o; o >>= 1) s += __shfl_xor(s, o);
  float m = s * (1.f / D), q = 0.f;
  for (int j = 0; j < 8; ++j) q += (v[j] - m) * (v[j] - m);
  for (int o = 32; o; o >>= 1) q += __shfl_xor(q, o);
  float r = rsqrtf(q * (1.f / D) + 1e-5f);
  for (int j = 0; j < 8; ++j) {
    int d = lane * 8 + j;
    h[row * D + d] = f2bf((v[j] - m) * r * (1.f + scale[d]) + shift[d]);
  }
}

template <bool BT, int EPI>
__global__ void k_gemm(const u16* A, int lda, const u16* B, int ldb, int K, float sign, void* Cv, int ldc,
                       const void* resid, const float* gate, const float* Cin) {
  __shared__ u16 sa[64][40], sb[64][40];
  int tm = blockIdx.y * 64, tn = blockIdx.x * 64;
  int t = threadIdx.x, lane = t & 63, w = t >> 6, wr = w >> 1, wc = w & 1;
  f32x4 acc[2][2] = {};
  for (int k0 = 0; k0 < K; k0 += 32) {
    { int r = t >> 2, cc = (t & 3) * 8;
      *(bf16x8*)&sa[r][cc] = *(const bf16x8*)(A + (size_t)(tm + r) * lda + k0 + cc);
      if (BT) *(bf16x8*)&sb[r][cc] = *(const bf16x8*)(B + (size_t)(tn + r) * ldb + k0 + cc);
      else { int kk = t >> 3, n8 = (t & 7) * 8;
        bf16x8 v = *(const bf16x8*)(B + (size_t)(k0 + kk) * ldb + tn + n8);
        for (int j = 0; j < 8; ++j) sb[n8 + j][kk] = v[j]; }
    }
    __syncthreads();
    bf16x8 a[2], b[2];
    for (int i = 0; i < 2; ++i) {
      a[i] = *(bf16x8*)&sa[wr * 32 + i * 16 + (lane & 15)][(lane >> 4) * 8];
      b[i] = *(bf16x8*)&sb[wc * 32 + i * 16 + (lane & 15)][(lane >> 4) * 8];
    }
    for (int i = 0; i < 2; ++i) for (int j = 0; j < 2; ++j)
      acc[i][j] = __builtin_amdgcn_mfma_f32_16x16x32_bf16(a[i], b[j], acc[i][j], 0, 0, 0);
    __syncthreads();
  }
  for (int i = 0; i < 2; ++i) for (int j = 0; j < 2; ++j) for (int r = 0; r < 4; ++r) {
    int row = tm + wr * 32 + i * 16 + (lane >> 4) * 4 + r, col = tn + wc * 32 + j * 16 + (lane & 15);
    float v = acc[i][j][r] * sign;
    size_t o = (size_t)row * ldc + col;
    if (EPI == 0) ((u16*)Cv)[o] = f2bf(v);
    if (EPI == 1) ((float*)Cv)[o] = v;
    if (EPI == 2) ((float*)Cv)[o] = bf2f(((const u16*)resid)[o]) + gate[col] * (v + Cin[o]);
    if (EPI == 3) ((u16*)Cv)[o] = f2bf(fmaxf(v, 0.f));
    if (EPI == 4) ((u16*)Cv)[o] = f2bf(((const float*)resid)[o] + gate[col] * v);
  }
}

#define NEXTRA 0
__global__ void k_empty(float* p) { if (p == nullptr) __builtin_trap(); }

extern "C" void kernel_launch(void* const* d_in, const int* in_sizes, int n_in, void* d_out, int out_size,
                              void* d_ws, size_t ws_size, hipStream_t stream) {
  const u16* x = (const u16*)d_in[0];
  const u16 *Sre = (const u16*)d_in[1], *Sim = (const u16*)d_in[2], *Dre = (const u16*)d_in[3], *Dim = (const u16*)d_in[4];
  const u16 *w1 = (const u16*)d_in[6], *w2 = (const u16*)d_in[7], *c = (const u16*)d_in[8], *wm = (const u16*)d_in[9];
  char* ws = (char*)d_ws;
  float* mod = (float*)ws;
  u16* h = (u16*)(ws + (1 << 20));
  u16* a = (u16*)(ws + (4 << 20));
  u16* b = (u16*)(ws + (8 << 20));
  float* y1 = (float*)(ws + (12 << 20));
  float* x1 = (float*)(ws + (16 << 20));
  u16* f = (u16*)(ws + (24 << 20));
  k_mod<<<48, 256, 0, stream>>>(c, wm, mod);
  k_ln<u16><<<S / 4, 256, 0, stream>>>(x, mod, mod + 512, h);
  k_gemm<true, 0><<<dim3(8, 32), 256, 0, stream>>>(h, D, Dre, D, D, 1.f, a, D, nullptr, nullptr, nullptr);
  k_gemm<true, 0><<<dim3(8, 32), 256, 0, stream>>>(h, D, Dim, D, D, 1.f, b, D, nullptr, nullptr, nullptr);
  k_gemm<false, 1><<<dim3(8, 32), 256, 0, stream>>>(Sre, S, a, D, S, 1.f, y1, D, nullptr, nullptr, nullptr);
  k_gemm<false, 2><<<dim3(8, 32), 256, 0, stream>>>(Sim, S, b, D, S, -1.f, x1, D, x, mod + 1024, y1);
  k_ln<float><<<S / 4, 256, 0, stream>>>(x1, mod + 1536, mod + 2048, h);
  k_gemm<false, 3><<<dim3(28, 32), 256, 0, stream>>>(h, D, w1, F, D, 1.f, f, F, nullptr, nullptr, nullptr);
  k_gemm<false, 4><<<dim3(8, 32), 256, 0, stream>>>(f, F, w2, D, F, 1.f, d_out, D, x1, mod + 2560, nullptr);
  for (int i = 0; i < NEXTRA; ++i) k_empty<<<256, 256, 0, stream>>>(mod);
}
```

```cpp
#include <hip/hip_runtime.h>
#include <stdint.h>

struct Desc {
  const void* p;
  uint32_t bytes;
  uint32_t fmt;
};
struct Args {
  Desc d[12];
};

__global__ __launch_bounds__(256, 1) void fused_block(Args args) {
#if defined(__HIP_DEVICE_COMPILE__)
  __shared__ __attribute__((aligned(1024))) char lds[163840];
  asm volatile(R"ASM(
  s_load_dwordx16 s[4:19], s[0:1], 0
  s_load_dwordx16 s[20:35], s[0:1], 64
  s_load_dwordx16 s[36:51], s[0:1], 0x80
  v_lshrrev_b32 v3, 6, v0
  v_and_b32 v1, 63, v0
  v_readfirstlane_b32 s58, v3
  v_lshlrev_b32 v2, 4, v1
  s_lshr_b32 s52, s2, 3
  s_and_b32 s59, s52, 7
  s_lshr_b32 s53, s52, 3
  s_and_b32 s54, s2, 7
  s_lshl2_add_u32 s60, s54, s53
  s_and_b32 s61, s59, 6
  s_mul_i32 s53, s58, 2
  s_add_u32 s61, s61, s53
  s_xor_b32 s62, s58, 3
  s_mul_i32 s63, s58, 0x9000
  s_mov_b32 s64, 0x30d40
  s_lshl2_add_u32 s52, s2, s58
  s_lshl_b32 s52, s52, 11
  v_lshrrev_b32 v3, 3, v1
  v_lshrrev_b32 v4, 4, v1
  v_and_b32 v5, 7, v1
  v_xor_b32 v5, v5, v4
  v_lshlrev_b32 v55, 4, v5
  v_lshl_or_b32 v51, v3, 12, v55
  v_xor_b32 v6, 64, v51
  v_add_u32 v52, 0x7c00, v6
  v_add_u32 v53, 0xf800, v51
  v_add_u32 v54, 0x17400, v6
  v_mov_b32 v7, 0
  s_lshl_b32 s53, s60, 18
  s_lshl_b32 s54, s58, 10
  s_add_u32 s53, s53, s54
  s_and_b32 s54, s62, 1
  s_lshl_b32 s54, s54, 17
  s_add_u32 s65, s53, s54
  s_xor_b32 s68, s65, 0x20000
  s_lshr_b32 s53, s2, 3
  s_lshl2_add_u32 s53, s53, s58
  s_lshl_b32 s53, s53, 8
  v_and_b32 v6, 1, v1
  v_lshl_add_u32 v6, v6, 7, s53
  s_waitcnt lgkmcnt(0)
  buffer_load_dwordx4 v[56:59], v2, s[4:7], s52 offen
  buffer_load_dwordx4 v[60:63], v2, s[4:7], s52 offen offset:1024
  buffer_load_dwordx2 v[18:19], v7, s[8:11], 0 offen sc1
  s_getpc_b64 s[48:49]
  s_and_b32 s48, s48, 0xffffff00
  s_mov_b32 s50, 0x7000
  buffer_load_dword v8, v6, s[48:51], 0 offen
  s_cmp_lt_u32 s58, 2
  s_mov_b64 s[56:57], s[20:21]
  s_cselect_b64 s[20:21], s[24:25], s[20:21]
  s_cselect_b64 s[24:25], s[56:57], s[24:25]
  s_add_u32 s69, s61, 0
  s_and_b32 s69, s69, 7
  s_lshl_b32 s69, s69, 7
  s_add_u32 m0, s63, 0
  s_add_u32 s70, s65, s69
  buffer_load_dwordx4 v51, s[20:23], s70 offen lds
  buffer_load_dwordx4 v52, s[20:23], s70 offen offset:1024 lds
  buffer_load_dwordx4 v53, s[20:23], s70 offen offset:2048 lds
  buffer_load_dwordx4 v54, s[20:23], s70 offen offset:3072 lds
  s_add_u32 s69, s61, 1
  s_and_b32 s69, s69, 7
  s_lshl_b32 s69, s69, 7
  s_add_u32 m0, s63, 0x4000
  s_add_u32 s70, s65, s69
  buffer_load_dwordx4 v51, s[20:23], s70 offen lds
  buffer_load_dwordx4 v52, s[20:23], s70 offen offset:1024 lds
  buffer_load_dwordx4 v53, s[20:23], s70 offen offset:2048 lds
  buffer_load_dwordx4 v54, s[20:23], s70 offen offset:3072 lds
  s_mov_b64 exec, 0
  s_cmp_eq_u32 s58, 0
  s_cbranch_scc1 LS0_%=
  s_cmp_eq_u32 s58, 1
  s_cbranch_scc1 LS1_%=
  s_cmp_eq_u32 s58, 2
  s_cbranch_scc1 LS2_%=
  s_branch LS3_%=
L1_%=:
  s_mov_b64 exec, -1
  s_waitcnt vmcnt(11)
  v_lshlrev_b32 v240, 16, v56
  s_add_u32 s69, s61, 0
  s_and_b32 s69, s69, 7
  s_lshl_b32 s69, s69, 7
  s_add_u32 m0, s63, 0x1000
  s_add_u32 s70, s68, s69
  buffer_load_dwordx4 v51, s[20:23], s70 offen lds
  buffer_load_dwordx4 v52, s[20:23], s70 offen offset:1024 lds
  buffer_load_dwordx4 v53, s[20:23], s70 offen offset:2048 lds
  buffer_load_dwordx4 v54, s[20:23], s70 offen offset:3072 lds
  v_and_b32 v241, 0xffff0000, v56
  v_lshlrev_b32 v242, 16, v57
  v_and_b32 v243, 0xffff0000, v57
  v_lshlrev_b32 v244, 16, v58
  v_and_b32 v245, 0xffff0000, v58
  v_lshlrev_b32 v246, 16, v59
  v_and_b32 v247, 0xffff0000, v59
  s_waitcnt vmcnt(14)
  v_lshlrev_b32 v248, 16, v60
  v_and_b32 v249, 0xffff0000, v60
  v_lshlrev_b32 v250, 16, v61
  v_and_b32 v251, 0xffff0000, v61
  v_lshlrev_b32 v252, 16, v62
  v_and_b32 v253, 0xffff0000, v62
  v_lshlrev_b32 v254, 16, v63
  v_and_b32 v255, 0xffff0000, v63
  v_pk_add_f32 v[64:65], v[240:241], v[242:243]
  v_pk_add_f32 v[66:67], v[244:245], v[246:247]
  v_pk_add_f32 v[64:65], v[64:65], v[66:67]
  v_add_f32 v64, v64, v65
  v_pk_add_f32 v[68:69], v[248:249], v[250:251]
  v_pk_add_f32 v[70:71], v[252:253], v[254:255]
  v_pk_add_f32 v[68:69], v[68:69], v[70:71]
  v_add_f32 v68, v68, v69
  s_mul_i32 s54, s58, 0x880
  v_add_f32_dpp v64, v64, v64 quad_perm:[1,0,3,2] row_mask:0xf bank_mask:0xf
  v_add_f32_dpp v68, v68, v68 quad_perm:[1,0,3,2] row_mask:0xf bank_mask:0xf
  s_add_u32 s54, s54, 0x24000
  v_add_f32_dpp v64, v64, v64 quad_perm:[2,3,0,1] row_mask:0xf bank_mask:0xf
  v_add_f32_dpp v68, v68, v68 quad_perm:[2,3,0,1] row_mask:0xf bank_mask:0xf
  v_add_u32 v74, s54, v2
  v_add_f32_dpp v64, v64, v64 row_half_mirror row_mask:0xf bank_mask:0xf
  v_add_f32_dpp v68, v68, v68 row_half_mirror row_mask:0xf bank_mask:0xf
  v_bfe_u32 v72, v1, 2, 2
  v_add_f32_dpp v64, v64, v64 row_mirror row_mask:0xf bank_mask:0xf
  v_add_f32_dpp v68, v68, v68 row_mirror row_mask:0xf bank_mask:0xf
  v_and_b32 v73, 48, v2
  v_add_f32_dpp v64, v64, v64 row_bcast:15 row_mask:0xa bank_mask:0xf
  v_add_f32_dpp v68, v68, v68 row_bcast:15 row_mask:0xa bank_mask:0xf
  v_mul_u32_u24 v75, 0x440, v72
  v_add_f32_dpp v64, v64, v64 row_bcast:31 row_mask:0xc bank_mask:0xf
  v_add_f32_dpp v68, v68, v68 row_bcast:31 row_mask:0xc bank_mask:0xf
  v_mul_f32 v64, 0xbb000000, v64
  v_mul_f32 v68, 0xbb000000, v68
  v_readlane_b32 s56, v64, 63
  v_readlane_b32 s57, v68, 63
  v_lshrrev_b32 v4, 5, v1
  s_nop 0
  v_pk_add_f32 v[240:241], v[240:241], s[56:57] op_sel:[0,0] op_sel_hi:[1,0]
  v_pk_add_f32 v[242:243], v[242:243], s[56:57] op_sel:[0,0] op_sel_hi:[1,0]
  v_pk_add_f32 v[244:245], v[244:245], s[56:57] op_sel:[0,0] op_sel_hi:[1,0]
  v_pk_add_f32 v[246:247], v[246:247], s[56:57] op_sel:[0,0] op_sel_hi:[1,0]
  v_pk_add_f32 v[248:249], v[248:249], s[56:57] op_sel:[0,1] op_sel_hi:[1,1]
  v_pk_add_f32 v[250:251], v[250:251], s[56:57] op_sel:[0,1] op_sel_hi:[1,1]
  v_pk_add_f32 v[252:253], v[252:253], s[56:57] op_sel:[0,1] op_sel_hi:[1,1]
  v_pk_add_f32 v[254:255], v[254:255], s[56:57] op_sel:[0,1] op_sel_hi:[1,1]
  v_pk_mul_f32 v[64:65], v[240:241], v[240:241]
  v_pk_fma_f32 v[64:65], v[242:243], v[242:243], v[64:65]
  v_pk_fma_f32 v[64:65], v[244:245], v[244:245], v[64:65]
  v_pk_fma_f32 v[64:65], v[246:247], v[246:247], v[64:65]
  v_add_f32 v64, v64, v65
  v_pk_mul_f32 v[68:69], v[248:249], v[248:249]
  v_pk_fma_f32 v[68:69], v[250:251], v[250:251], v[68:69]
  v_pk_fma_f32 v[68:69], v[252:253], v[252:253], v[68:69]
  v_pk_fma_f32 v[68:69], v[254:255], v[254:255], v[68:69]
  v_add_f32 v68, v68, v69
  v_mov_b32 v6, 0x3727c5ac
  v_lshl_add_u32 v75, v4, 6, v75
  v_add_f32_dpp v64, v64, v64 quad_perm:[1,0,3,2] row_mask:0xf bank_mask:0xf
  v_add_f32_dpp v68, v68, v68 quad_perm:[1,0,3,2] row_mask:0xf bank_mask:0xf
  v_bfe_u32 v4, v1, 4, 1
  v_add_f32_dpp v64, v64, v64 quad_perm:[2,3,0,1] row_mask:0xf bank_mask:0xf
  v_add_f32_dpp v68, v68, v68 quad_perm:[2,3,0,1] row_mask:0xf bank_mask:0xf
  v_lshl_add_u32 v75, v4, 3, v75
LS0_%=:
  s_cbranch_execz L1_%=
  v_add_f32_dpp v64, v64, v64 row_half_mirror row_mask:0xf bank_mask:0xf
  v_add_f32_dpp v68, v68, v68 row_half_mirror row_mask:0xf bank_mask:0xf
  s_lshl_b32 s54, s58, 8
  v_add_f32_dpp v64, v64, v64 row_mirror row_mask:0xf bank_mask:0xf
  v_add_f32_dpp v68, v68, v68 row_mirror row_mask:0xf bank_mask:0xf
  s_add_u32 s54, s54, 0x24000
  v_add_f32_dpp v64, v64, v64 row_bcast:15 row_mask:0xa bank_mask:0xf
  v_add_f32_dpp v68, v68, v68 row_bcast:15 row_mask:0xa bank_mask:0xf
  v_add3_u32 v75, v75, v73, s54
  v_add_f32_dpp v64, v64, v64 row_bcast:31 row_mask:0xc bank_mask:0xf
  v_add_f32_dpp v68, v68, v68 row_bcast:31 row_mask:0xc bank_mask:0xf
  v_fmamk_f32 v64, v64, 0x3b000000, v6
  v_rsq_f32 v64, v64
  v_fmamk_f32 v68, v68, 0x3b000000, v6
  v_rsq_f32 v68, v68
  v_readlane_b32 s56, v64, 63
  v_readlane_b32 s57, v68, 63
  v_lshrrev_b32 v4, 4, v1
  s_nop 0
  v_pk_mul_f32 v[240:241], v[240:241], s[56:57] op_sel:[0,0] op_sel_hi:[1,0]
  v_pk_mul_f32 v[242:243], v[242:243], s[56:57] op_sel:[0,0] op_sel_hi:[1,0]
  v_pk_mul_f32 v[244:245], v[244:245], s[56:57] op_sel:[0,0] op_sel_hi:[1,0]
  v_pk_mul_f32 v[246:247], v[246:247], s[56:57] op_sel:[0,0] op_sel_hi:[1,0]
  v_cvt_pk_bf16_f32 v56, v240, v241
  v_cvt_pk_bf16_f32 v57, v242, v243
  v_cvt_pk_bf16_f32 v58, v244, v245
  v_cvt_pk_bf16_f32 v59, v246, v247
  ds_write_b128 v74, v[56:59]
  v_pk_mul_f32 v[248:249], v[248:249], s[56:57] op_sel:[0,1] op_sel_hi:[1,1]
  v_pk_mul_f32 v[250:251], v[250:251], s[56:57] op_sel:[0,1] op_sel_hi:[1,1]
  v_pk_mul_f32 v[252:253], v[252:253], s[56:57] op_sel:[0,1] op_sel_hi:[1,1]
  v_pk_mul_f32 v[254:255], v[254:255], s[56:57] op_sel:[0,1] op_sel_hi:[1,1]
  v_cvt_pk_bf16_f32 v60, v248, v249
  v_cvt_pk_bf16_f32 v61, v250, v251
  v_cvt_pk_bf16_f32 v62, v252, v253
  v_cvt_pk_bf16_f32 v63, v254, v255
  ds_write_b128 v74, v[60:63] offset:1088
  v_and_b32 v5, 0xf0, v2
  v_lshl_or_b32 v76, v4, 10, v5
  s_lshl_b32 s54, s58, 19
  s_lshr_b32 s55, s2, 2
  s_lshl_b32 s55, s55, 12
  s_add_u32 s54, s54, s55
  s_and_b32 s55, s2, 3
  s_lshl_b32 s55, s55, 8
  s_add_u32 s54, s54, s55
  s_add_u32 s48, s54, 0x100000
  s_add_u32 s49, s48, 0x40000
  s_waitcnt lgkmcnt(0)
  s_barrier
  ds_read_b64_tr_b16 v[56:57], v75
  ds_read_b64_tr_b16 v[58:59], v75 offset:4352
  ds_read_b64_tr_b16 v[60:61], v75 offset:128
  ds_read_b64_tr_b16 v[62:63], v75 offset:4480
  s_waitcnt lgkmcnt(2)
  buffer_store_dwordx4 v[56:59], v76, s[8:11], s48 offen sc1
  s_waitcnt lgkmcnt(0)
  buffer_store_dwordx4 v[60:63], v76, s[8:11], s49 offen sc1
  s_add_u32 s69, s61, 1
  s_and_b32 s69, s69, 7
  s_lshl_b32 s69, s69, 7
  s_add_u32 m0, s63, 0x5000
  s_add_u32 s70, s68, s69
  buffer_load_dwordx4 v51, s[20:23], s70 offen lds
  buffer_load_dwordx4 v52, s[20:23], s70 offen offset:1024 lds
  buffer_load_dwordx4 v53, s[20:23], s70 offen offset:2048 lds
  buffer_load_dwordx4 v54, s[20:23], s70 offen offset:3072 lds
  s_add_u32 s69, s61, 0
  s_and_b32 s69, s69, 7
  s_lshl_b32 s69, s69, 7
  s_add_u32 m0, s63, 0x2000
  s_add_u32 s70, s65, s69
  buffer_load_dwordx4 v51, s[24:27], s70 offen lds
  buffer_load_dwordx4 v52, s[24:27], s70 offen offset:1024 lds
  buffer_load_dwordx4 v53, s[24:27], s70 offen offset:2048 lds
  buffer_load_dwordx4 v54, s[24:27], s70 offen offset:3072 lds
  s_waitcnt vmcnt(23)
  v_readfirstlane_b32 s66, v18
  v_readfirstlane_b32 s67, v19
  v_cmp_gt_u32 vcc, 8, v0
  v_mov_b32 v5, 0x40000
  s_add_u32 s66, s66, 1
  v_cndmask_b32 v9, v5, v0, vcc
  v_mov_b32 v18, s66
  v_lshlrev_b32 v64, 12, v9
  v_lshlrev_b32 v9, 7, v9
  s_lshl_b32 s48, s2, 4
  s_add_u32 s48, s48, 0x1000
  s_mul_i32 s52, s2, 0x556
  s_lshr_b32 s52, s52, 16
  s_mul_i32 s53, s52, 48
  s_sub_u32 s53, s2, s53
  s_mul_i32 s54, s52, 0x3000
  s_lshl_b32 s55, s53, 8
  s_add_u32 s54, s54, s55
  s_lshl_b32 s55, s58, 6
  s_add_u32 s54, s54, s55
  s_add_u32 s49, s54, 0x40000
  v_and_b32 v3, 3, v1
  v_and_b32 v4, 48, v1
  v_lshl_or_b32 v3, v3, 6, v4
  s_lshl_b32 s54, s52, 8
  v_add_u32 v65, s54, v3
  v_lshrrev_b32 v3, 3, v1
  v_mul_u32_u24 v3, 0x1800, v3
  v_and_b32 v4, 0x70, v2
  s_lshl2_add_u32 s54, s52, s58
  s_mul_i32 s54, s54, 0x30000
  s_lshl_b32 s55, s53, 7
  s_add_u32 s54, s54, s55
  v_add3_u32 v66, v3, v4, s54
  s_add_u32 m0, s63, 0x8000
  s_mov_b64 exec, 0
  s_cmp_eq_u32 s58, 0
  s_cbranch_scc1 LS4_%=
  s_cmp_eq_u32 s58, 1
  s_cbranch_scc1 LS5_%=
  s_cmp_eq_u32 s58, 2
  s_cbranch_scc1 LS6_%=
  s_branch LS7_%=
L2_%=:
  s_mov_b64 exec, -1
  s_waitcnt vmcnt(8)
  s_barrier
  buffer_store_dwordx2 v[18:19], v64, s[8:11], s48 offen sc1
  buffer_load_dwordx4 v[56:59], v65, s[16:19], 0 offen
  buffer_load_dwordx4 v66, s[12:15], 0 offen lds
  v_add_u32 v66, 0xbc00, v66
  buffer_load_dwordx4 v66, s[12:15], 0 offen offset:1024 lds
  v_add_u32 v66, 0xbc00, v66
  buffer_load_dwordx4 v66, s[12:15], 0 offen offset:2048 lds
  v_add_u32 v66, 0xbc00, v66
  buffer_load_dwordx4 v66, s[12:15], 0 offen offset:3072 lds
  s_and_b32 s52, s2, 7
  s_lshr_b32 s53, s52, 1
  s_lshl_b32 s53, s53, 3
  s_lshr_b32 s54, s2, 5
  s_add_u32 s12, s53, s54
  s_and_b32 s52, s52, 1
  s_lshl_b32 s52, s52, 2
  s_lshr_b32 s53, s2, 3
  s_and_b32 s53, s53, 3
  s_add_u32 s13, s52, s53
  v_and_b32 v3, 15, v1
  s_add_u32 s69, s61, 1
  v_bfe_u32 v4, v1, 1, 3
  s_and_b32 s69, s69, 7
  v_lshrrev_b32 v5, 4, v1
  s_lshl_b32 s69, s69, 7
  v_xor_b32 v4, v4, v5
  s_add_u32 m0, s63, 0x6000
  v_lshlrev_b32 v4, 4, v4
  s_add_u32 s70, s65, s69
  v_lshl_or_b32 v3, v3, 7, v4
  buffer_load_dwordx4 v51, s[24:27], s70 offen lds
  v_add_u32 v49, s63, v3
  buffer_load_dwordx4 v52, s[24:27], s70 offen offset:1024 lds
  v_xor_b32 v50, 64, v49
  v_add_u32 v10, 0x1000, v2
  v_and_b32 v3, 3, v1
  v_mov_b32 v4, 0x3f803f80
  v_cmp_eq_u32 vcc, 0, v3
  s_nop 1
  v_cndmask_b32 v5, v7, v4, vcc
  buffer_load_dwordx4 v53, s[24:27], s70 offen offset:2048 lds
  v_accvgpr_write_b32 a48, v5
  v_accvgpr_write_b32 a49, v5
  v_accvgpr_write_b32 a50, v5
  v_accvgpr_write_b32 a51, v5
  v_cmp_eq_u32 vcc, 1, v3
  s_nop 1
LS1_%=:
  s_cbranch_execz L1_%=
  v_cndmask_b32 v5, v7, v4, vcc
  v_accvgpr_write_b32 a52, v5
  v_accvgpr_write_b32 a53, v5
  v_accvgpr_write_b32 a54, v5
  v_accvgpr_write_b32 a55, v5
  buffer_load_dwordx4 v54, s[24:27], s70 offen offset:3072 lds
  v_and_b32 v3, 1, v1
  s_add_u32 s69, s61, 0
  v_lshlrev_b32 v3, 4, v3
  s_and_b32 s69, s69, 7
  v_mov_b32 v4, 0x3f80
  s_lshl_b32 s69, s69, 7
  v_lshlrev_b32 v4, v3, v4
  s_add_u32 m0, s63, 0x3000
  v_lshrrev_b32 v3, 4, v1
  s_add_u32 s70, s68, s69
  v_bfe_u32 v5, v1, 1, 3
  v_lshl_add_u32 v6, v3, 1, 0
  buffer_load_dwordx4 v51, s[24:27], s70 offen lds
  v_cmp_eq_u32 vcc, v6, v5
  v_accvgpr_write_b32 a56, 0
  v_accvgpr_write_b32 a60, 0
  v_cndmask_b32 v6, v7, v4, vcc
  v_accvgpr_write_b32 a58, v6
  v_lshl_add_u32 v6, v3, 1, 1
  v_cmp_eq_u32 vcc, v6, v5
  v_accvgpr_write_b32 a57, 0
  v_accvgpr_write_b32 a61, 0
  v_cndmask_b32 v6, v7, v4, vcc
  v_accvgpr_write_b32 a59, v6
  s_lshl2_add_u32 s52, s59, s58
  s_lshl_b32 s52, s52, 16
  s_add_u32 s14, s52, 0x100000
  v_lshrrev_b32 v3, 3, v1
  buffer_load_dwordx4 v52, s[24:27], s70 offen offset:1024 lds
  v_lshl_or_b32 v67, v3, 10, v55
  v_xor_b32 v3, 64, v67
  v_add_u32 v68, 0x1c00, v3
  v_add_u32 v69, 0x3800, v67
  v_add_u32 v70, 0x5400, v3
  s_and_b32 s52, s62, 1
  s_lshl1_add_u32 s52, s13, s52
  s_lshl_b32 s52, s52, 15
  s_lshl_b32 s53, s58, 8
  s_add_u32 s16, s52, s53
  v_add_u32 v11, s63, v2
  buffer_load_dwordx4 v53, s[24:27], s70 offen offset:2048 lds
  v_lshlrev_b32 v55, 3, v0
  v_add_u32 v55, 0x24000, v55
  s_xor_b32 s52, s62, 0
  s_mul_i32 s53, s52, 0x9000
  v_add_u32 v46, s53, v2
  s_lshl_b32 s53, s52, 9
  s_add_u32 s53, s53, 0x24000
  v_lshl_add_u32 v74, v1, 3, s53
  s_xor_b32 s52, s62, 1
  s_mul_i32 s53, s52, 0x9000
  v_add_u32 v47, s53, v2
  buffer_load_dwordx4 v54, s[24:27], s70 offen offset:3072 lds
  s_lshl_b32 s53, s52, 9
  s_add_u32 s53, s53, 0x24800
  v_lshl_add_u32 v75, v1, 3, s53
  s_add_u32 s69, s61, 1
  s_xor_b32 s52, s62, 2
  s_mul_i32 s53, s52, 0x9000
  v_add_u32 v48, s53, v2
  s_and_b32 s69, s69, 7
  s_lshl_b32 s53, s52, 9
  s_add_u32 s53, s53, 0x25000
  v_lshl_add_u32 v76, v1, 3, s53
  s_lshl_b32 s69, s69, 7
  s_lshl_b32 s52, s60, 17
  s_and_b32 s53, s58, 1
  s_lshl_b32 s53, s53, 16
  s_add_u32 s52, s52, s53
  s_lshr_b32 s53, s58, 1
  s_lshl1_add_u32 s53, s59, s53
  s_lshl_b32 s53, s53, 11
  s_add_u32 s52, s52, s53
  s_add_u32 s18, s52, 0x300000
  s_add_u32 s19, s18, 0x8000
  v_and_b32 v12, 7, v1
  s_add_u32 m0, s63, 0x7000
  v_lshlrev_b32 v12, 3, v12
  s_add_u32 s70, s68, s69
  s_lshl_b32 s52, s60, 10
  s_add_u32 s52, s52, 0xa000
  s_lshl3_add_u32 s50, s59, s52
  s_lshl_b32 s52, s12, 10
  s_add_u32 s52, s52, 0xa000
  s_lshl3_add_u32 s71, s13, s52
  s_lshl_b32 s53, s13, 7
  s_add_u32 s51, s52, s53
  v_lshrrev_b32 v6, 3, v1
  buffer_load_dwordx4 v51, s[24:27], s70 offen lds
  v_lshrrev_b32 v4, 4, v1
  buffer_load_dwordx4 v52, s[24:27], s70 offen offset:1024 lds
  v_lshlrev_b32 v4, 5, v4
  v_and_b32 v5, 0x70, v2
  v_xor_b32 v4, v4, v5
  v_mul_u32_u24 v3, 0xe00, v6
  s_cmp_lt_u32 s13, 7
  s_cselect_b32 s52, 0, 0x40000000
  v_add3_u32 v30, v3, v4, s52
  v_add_u32 v13, s52, v2
  buffer_load_dwordx4 v53, s[24:27], s70 offen offset:2048 lds
  v_lshl_or_b32 v34, v6, 10, v4
  s_lshl2_add_u32 s52, s13, s58
  s_lshl_b32 s72, s52, 7
  s_mul_i32 s73, s58, 0x70000
  s_lshl_b32 s52, s13, 7
  s_add_u32 s73, s73, s52
  s_sub_u32 s72, s72, 0x1c000
  s_sub_u32 s73, s73, 0x8000
  v_add_u32 v31, 0x6c00, v30
  v_add_u32 v32, 0xd800, v30
  v_add_u32 v33, 0x14400, v30
  v_add_u32 v35, 0x1c00, v34
  buffer_load_dwordx4 v54, s[24:27], s70 offen offset:3072 lds
  v_add_u32 v36, 0x3800, v34
  v_add_u32 v37, 0x5400, v34
  v_lshrrev_b32 v3, 4, v1
  v_lshl_add_u32 v3, v3, 3, v72
  v_lshrrev_b32 v4, 1, v73
  v_lshl_or_b32 v65, v3, 7, v4
  v_lshrrev_b32 v3, 2, v1
  v_lshl_or_b32 v3, v3, 7, v4
  v_bfe_u32 v5, v1, 3, 2
  v_lshl_or_b32 v3, v5, 5, v3
  v_add_u32 v38, s63, v3
  v_xor_b32 v39, 32, v38
  v_xor_b32 v40, 64, v38
  v_xor_b32 v41, 0x60, v38
  s_and_b32 s52, s62, 1
  s_lshl_b32 s52, s52, 6
  v_xor_b32 v42, s52, v38
  v_xor_b32 v43, s52, v39
  v_xor_b32 v44, s52, v40
  v_xor_b32 v45, s52, v41
  s_lshl_b32 s52, s58, 5
  s_add_u32 s52, s52, 0x8000
  v_add_u32 v65, s52, v65
  v_and_b32 v4, 3, v1
  v_cmp_eq_u32 vcc, 0, v4
  v_bfe_u32 v4, v1, 2, 1
  v_lshlrev_b32 v4, 10, v4
  v_sub_u32 v4, 0x26800, v4
  v_mov_b32 v5, 0x26c00
  v_cndmask_b32 v4, v5, v4, vcc
  v_and_b32 v5, 48, v1
  v_add_u32 v14, v5, v4
  v_and_b32 v3, 15, v1
  v_bfe_u32 v4, v1, 4, 1
  v_lshrrev_b32 v5, 5, v1
  v_lshl_or_b32 v4, v4, 1, v5
  v_lshlrev_b32 v4, 4, v4
  v_lshl_or_b32 v15, v3, 10, v4
  s_lshr_b32 s76, s58, 1
  s_lshl2_add_u32 s52, s12, s76
  s_lshl_b32 s52, s52, 14
  s_and_b32 s54, s58, 1
  s_lshl1_add_u32 s53, s13, s54
  s_lshl_b32 s53, s53, 6
  s_add_u32 s74, s52, s53
  s_add_u32 s75, s74, 0x8000
  v_lshrrev_b32 v4, 5, v1
  v_lshlrev_b32 v3, 3, v3
  v_lshl_or_b32 v3, v4, 8, v3
  v_bfe_u32 v5, v1, 4, 1
  v_lshl_or_b32 v64, v5, 2, v3
  s_lshl_b32 s52, s12, 17
  s_lshl_b32 s53, s58, 13
  s_add_u32 s52, s52, s53
  s_add_u32 s52, s52, 0x300000
  s_xor_b32 s53, s76, 1
  s_mul_i32 s54, s53, 0x8000
  s_add_u32 s78, s52, s54
  s_mul_i32 s54, s76, 0x8000
LS2_%=:
  s_cbranch_execz L1_%=
  s_add_u32 s79, s52, s54
  s_lshl_b32 s52, s12, 16
  s_lshl_b32 s54, s76, 14
  s_add_u32 s52, s52, s54
  s_and_b32 s54, s58, 1
  s_lshl1_add_u32 s55, s12, s54
  s_lshl_b32 s55, s55, 10
  s_add_u32 s81, s55, 0x60000
  s_lshl1_add_u32 s54, s13, s54
  s_lshl_b32 s55, s54, 10
  s_add_u32 s52, s52, s55
  s_add_u32 s82, s52, 0x700000
  s_add_u32 s83, s82, 0x8000
  s_lshl_b32 s52, s12, 13
  s_lshl_b32 s54, s54, 9
  s_add_u32 s52, s52, s54
  s_lshl_b32 s53, s76, 7
  s_add_u32 s52, s52, s53
  s_add_u32 s80, s52, 0x900000
  s_lshl_b32 s52, s58, 1
  v_add_u32 v4, s52, v4
  v_lshrrev_b32 v5, v4, 52
  v_and_b32 v5, 1, v5
  v_cmp_eq_u32 vcc, 1, v5
  v_mov_b32 v5, s59
  v_mov_b32 v6, s13
  v_cndmask_b32 v6, v5, v6, vcc
  v_cmp_gt_u32 vcc, 6, v4
  v_bfe_u32 v5, v1, 3, 2
  v_mul_u32_u24 v5, 48, v5
  v_cndmask_b32 v3, 4, v4, vcc
  v_lshl_add_u32 v6, v3, 3, v6
  v_add_lshl_u32 v77, v6, v5, 4
  v_and_b32 v3, 31, v1
  v_lshlrev_b32 v5, 3, v3
  v_lshl_or_b32 v78, v6, 8, v5
  s_mov_b32 s52, 0xd2
  v_lshrrev_b32 v5, v4, s52
  v_and_b32 v5, 1, v5
  v_cvt_f32_u32 v79, v5
  v_lshlrev_b32 v80, 3, v0
  v_add_u32 v80, 0x27000, v80
  v_and_b32 v3, 17, v1
  v_bfe_u32 v5, v1, 1, 2
  v_lshl_or_b32 v3, v5, 2, v3
  v_bfe_u32 v5, v1, 3, 1
  v_lshl_or_b32 v3, v5, 1, v3
  v_lshlrev_b32 v3, 2, v3
  v_lshrrev_b32 v5, 2, v4
  v_lshl_or_b32 v3, v5, 10, v3
  s_mov_b32 s52, 0x48
  v_lshrrev_b32 v5, v4, s52
  v_and_b32 v5, 1, v5
  v_cmp_eq_u32 vcc, 1, v5
  v_mov_b32 v5, 0x40000000
  s_nop 0
  v_cndmask_b32 v81, v5, v3, vcc
  v_lshrrev_b32 v3, 4, v1
  s_lshr_b32 s52, s58, 1
  s_lshl_b32 s52, s52, 11
  s_add_u32 s52, s52, 0x27000
  v_lshl_add_u32 v71, v3, 5, s52
  s_and_b32 s52, s58, 1
  s_lshl_b32 s52, s52, 7
  s_add_u32 s52, s52, 0x27000
  v_lshl_add_u32 v72, v3, 4, s52
  v_lshlrev_b32 v3, 2, v0
  v_add_u32 v3, 0x26c00, v3
  ds_write_b32 v3, v7
  v_cmp_gt_u32 vcc, 16, v1
  v_lshlrev_b32 v4, 2, v1
  s_nop 0
  v_cndmask_b32 v66, v5, v4, vcc
  s_mov_b64 exec, 0
  s_cmp_eq_u32 s58, 0
  s_cbranch_scc1 LS8_%=
  s_cmp_eq_u32 s58, 1
  s_cbranch_scc1 LS9_%=
  s_cmp_eq_u32 s58, 2
  s_cbranch_scc1 LS10_%=
  s_branch LS11_%=
L3_%=:
  s_mov_b64 exec, -1
  s_waitcnt vmcnt(16)
  v_lshlrev_b32 v240, 16, v56
  v_and_b32 v241, 0xffff0000, v56
  v_lshlrev_b32 v242, 16, v57
  v_and_b32 v243, 0xffff0000, v57
  v_lshlrev_b32 v244, 16, v58
  v_and_b32 v245, 0xffff0000, v58
  v_lshlrev_b32 v246, 16, v59
  v_and_b32 v247, 0xffff0000, v59
  v_mul_f32 v248, 0xbfb8aa3b, v240
  v_mul_f32 v249, 0xbfb8aa3b, v241
  v_mul_f32 v250, 0xbfb8aa3b, v242
  v_mul_f32 v251, 0xbfb8aa3b, v243
  v_mul_f32 v252, 0xbfb8aa3b, v244
  v_mul_f32 v253, 0xbfb8aa3b, v245
  v_mul_f32 v254, 0xbfb8aa3b, v246
  v_mul_f32 v255, 0xbfb8aa3b, v247
  v_exp_f32 v248, v248
  v_exp_f32 v249, v249
  v_exp_f32 v250, v250
  v_exp_f32 v251, v251
  v_exp_f32 v252, v252
  v_exp_f32 v253, v253
  v_exp_f32 v254, v254
  v_exp_f32 v255, v255
  v_add_f32 v248, 1.0, v248
  v_add_f32 v249, 1.0, v249
  v_add_f32 v250, 1.0, v250
  v_add_f32 v251, 1.0, v251
  v_add_f32 v252, 1.0, v252
  v_add_f32 v253, 1.0, v253
  v_add_f32 v254, 1.0, v254
  v_add_f32 v255, 1.0, v255
  v_rcp_f32 v248, v248
  v_rcp_f32 v249, v249
  v_rcp_f32 v250, v250
  v_rcp_f32 v251, v251
  v_rcp_f32 v252, v252
  v_rcp_f32 v253, v253
  v_rcp_f32 v254, v254
  v_rcp_f32 v255, v255
  v_mul_f32 v240, v240, v248
  v_mul_f32 v241, v241, v249
  v_mul_f32 v242, v242, v250
  v_mul_f32 v243, v243, v251
  v_mul_f32 v244, v244, v252
  v_mul_f32 v245, v245, v253
  v_mul_f32 v246, v246, v254
  v_mul_f32 v247, v247, v255
  v_cvt_pk_bf16_f32 v56, v240, v241
  v_cvt_pk_bf16_f32 v57, v242, v243
  v_cvt_pk_bf16_f32 v58, v244, v245
  v_cvt_pk_bf16_f32 v59, v246, v247
  s_lshl2_add_u32 s54, s2, s58
  s_lshl_b32 s55, s54, 9
  v_and_b32 v3, 3, v1
  v_lshlrev_b32 v3, 7, v3
  buffer_load_dword v8, v3, s[28:31], s55 offen
  buffer_load_dword v8, v3, s[32:35], s55 offen
  v_and_b32 v3, 3, v1
  v_lshlrev_b32 v3, 15, v3
  buffer_load_dword v8, v3, s[8:11], s51 offen sc1
  s_waitcnt vmcnt(15)
  s_barrier
  ds_read_b64_tr_b16 v[224:225], v65
  ds_read_b64_tr_b16 v[226:227], v65 offset:512
  ds_read_b64_tr_b16 v[228:229], v65 offset:36864
  ds_read_b64_tr_b16 v[230:231], v65 offset:37376
  v_add_u32 v65, 0x12000, v65
  ds_read_b64_tr_b16 v[232:233], v65
  ds_read_b64_tr_b16 v[234:235], v65 offset:512
  ds_read_b64_tr_b16 v[236:237], v65 offset:36864
  ds_read_b64_tr_b16 v[238:239], v65 offset:37376
  s_waitcnt lgkmcnt(6)
  v_mfma_f32_16x16x32_bf16 v[240:243], v[56:59], v[224:227], 0
  s_waitcnt lgkmcnt(4)
  v_mfma_f32_16x16x32_bf16 v[244:247], v[56:59], v[228:231], 0
  s_waitcnt lgkmcnt(2)
  v_mfma_f32_16x16x32_bf16 v[248:251], v[56:59], v[232:235], 0
  s_waitcnt lgkmcnt(0)
  v_mfma_f32_16x16x32_bf16 v[252:255], v[56:59], v[236:239], 0
  v_cmp_eq_u32 vcc, 0, v0
  v_mov_b32 v3, 0x40000000
  s_add_u32 s48, s48, 0x8000
  v_cndmask_b32 v65, v3, v7, vcc
  v_add_f32 v73, v240, v245
  s_nop 2
  v_add_f32 v250, v250, v255
  v_add_f32 v73, v73, v250
  s_and_b32 s52, s2, 7
  s_lshl2_add_u32 s52, s52, s58
  s_lshl_b32 s52, s52, 10
  s_add_u32 s52, s52, 0x1000
  ds_read_b128 a[0:3], v49
  ds_read_b128 a[4:7], v50
  ds_read_b128 a[8:11], v49 offset:2048
  ds_read_b128 a[12:15], v50 offset:2048
  s_mov_b64 exec, 0
  s_cmp_eq_u32 s58, 0
  s_cbranch_scc1 LS16_%=
  s_cmp_eq_u32 s58, 1
  s_cbranch_scc1 LS17_%=
  s_cmp_eq_u32 s58, 2
  s_cbranch_scc1 LS18_%=
  s_branch LS19_%=
L4_%=:
  s_mov_b64 exec, -1
L5_%=:
  buffer_load_dwordx2 v[20:21], v2, s[8:11], s52 offen sc1
  s_waitcnt vmcnt(0)
  v_cmp_ne_u64 vcc, s[66:67], v[20:21]
LS3_%=:
  s_cbranch_execz L1_%=
  s_cmp_eq_u64 vcc, 0
  s_cbranch_scc1 L6_%=
  s_sub_u32 s64, s64, 1
  s_cbranch_scc0 L5_%=
  s_mov_b32 s64, 0
L6_%=:
  s_add_u32 s15, s61, 0
  s_and_b32 s15, s15, 7
  s_lshl_b32 s15, s15, 13
  s_add_u32 s15, s15, s14
  buffer_load_dwordx4 a[96:99], v2, s[8:11], s15 offen sc1
  buffer_load_dwordx4 a[112:115], v10, s[8:11], s15 offen sc1
  buffer_load_dwordx4 a[100:103], v2, s[8:11], s15 offen offset:1024 sc1
  buffer_load_dwordx4 a[116:119], v10, s[8:11], s15 offen offset:1024 sc1
  buffer_load_dwordx4 a[104:107], v2, s[8:11], s15 offen offset:2048 sc1
  buffer_load_dwordx4 a[120:123], v10, s[8:11], s15 offen offset:2048 sc1
  buffer_load_dwordx4 a[108:111], v2, s[8:11], s15 offen offset:3072 sc1
  buffer_load_dwordx4 a[124:127], v10, s[8:11], s15 offen offset:3072 sc1
  buffer_store_dword v73, v66, s[8:11], s49 offen sc1
  s_lshr_b32 s52, s2, 3
  s_lshl2_add_u32 s52, s52, s58
  s_and_b32 s53, s2, 1
  s_lshl_b32 s54, s53, 18
  s_lshl_b32 s55, s52, 11
  s_add_u32 s84, s54, s55
  s_lshl_b32 s53, s53, 11
  s_lshr_b32 s54, s52, 4
  s_mul_i32 s54, s54, 0xe00
  s_and_b32 s55, s52, 15
  s_lshl_b32 s55, s55, 7
  s_add_u32 s53, s53, s54
  s_add_u32 s85, s53, s55
  v_lshrrev_b32 v3, 2, v1
  v_lshl_add_u32 v107, v3, 7, s84
  v_mul_u32_u24 v3, 0x7000, v3
  v_add_u32 v108, s85, v3
  s_waitcnt vmcnt(8) lgkmcnt(3)
  v_mfma_f32_16x16x32_bf16 v[128:131], a[96:99], a[0:3], 0
  s_add_u32 s15, s61, 1
  s_waitcnt vmcnt(7) lgkmcnt(2)
  v_mfma_f32_16x16x32_bf16 v[128:131], a[112:115], a[4:7], v[128:131]
  s_waitcnt vmcnt(6)
  v_mfma_f32_16x16x32_bf16 v[132:135], a[100:103], a[0:3], 0
  s_and_b32 s15, s15, 7
  s_waitcnt vmcnt(5)
  v_mfma_f32_16x16x32_bf16 v[132:135], a[116:119], a[4:7], v[132:135]
  s_waitcnt vmcnt(4)
  v_mfma_f32_16x16x32_bf16 v[136:139], a[104:107], a[0:3], 0
  s_lshl_b32 s15, s15, 13
  s_waitcnt vmcnt(3)
  v_mfma_f32_16x16x32_bf16 v[136:139], a[120:123], a[4:7], v[136:139]
  s_waitcnt vmcnt(2)
  v_mfma_f32_16x16x32_bf16 v[140:143], a[108:111], a[0:3], 0
  s_add_u32 s15, s15, s14
  s_waitcnt vmcnt(1)
  v_mfma_f32_16x16x32_bf16 v[140:143], a[124:127], a[4:7], v[140:143]
  v_mfma_f32_16x16x32_bf16 a[32:35], a[48:51], a[0:3], 0
  ds_read_b128 a[16:19], v49 offset:4096
  v_mfma_f32_16x16x32_bf16 a[32:35], a[48:51], a[4:7], a[32:35]
  buffer_load_dwordx4 a[128:131], v2, s[8:11], s15 offen sc1
  s_waitcnt lgkmcnt(2)
  v_mfma_f32_16x16x32_bf16 v[144:147], a[96:99], a[8:11], 0
  ds_read_b128 a[20:23], v50 offset:4096
  s_waitcnt lgkmcnt(2)
  v_mfma_f32_16x16x32_bf16 v[144:147], a[112:115], a[12:15], v[144:147]
  buffer_load_dwordx4 a[144:147], v10, s[8:11], s15 offen sc1
  v_mfma_f32_16x16x32_bf16 v[148:151], a[100:103], a[8:11], 0
  ds_read_b128 a[24:27], v49 offset:6144
  v_mfma_f32_16x16x32_bf16 v[148:151], a[116:119], a[12:15], v[148:151]
  buffer_load_dwordx4 a[132:135], v2, s[8:11], s15 offen offset:1024 sc1
  v_mfma_f32_16x16x32_bf16 v[152:155], a[104:107], a[8:11], 0
  ds_read_b128 a[28:31], v50 offset:6144
  v_mfma_f32_16x16x32_bf16 v[152:155], a[120:123], a[12:15], v[152:155]
  buffer_load_dwordx4 a[148:151], v10, s[8:11], s15 offen offset:1024 sc1
  v_mfma_f32_16x16x32_bf16 v[156:159], a[108:111], a[8:11], 0
  buffer_load_dwordx4 a[136:139], v2, s[8:11], s15 offen offset:2048 sc1
  v_mfma_f32_16x16x32_bf16 v[156:159], a[124:127], a[12:15], v[156:159]
  v_mfma_f32_16x16x32_bf16 a[32:35], a[52:55], a[8:11], a[32:35]
  buffer_load_dwordx4 a[152:155], v10, s[8:11], s15 offen offset:2048 sc1
  v_mfma_f32_16x16x32_bf16 a[32:35], a[52:55], a[12:15], a[32:35]
  s_waitcnt lgkmcnt(0)
  v_mfma_f32_16x16x32_bf16 v[160:163], a[96:99], a[16:19], 0
  buffer_load_dwordx4 a[140:143], v2, s[8:11], s15 offen offset:3072 sc1
  v_mfma_f32_16x16x32_bf16 v[160:163], a[112:115], a[20:23], v[160:163]
  v_mfma_f32_16x16x32_bf16 v[164:167], a[100:103], a[16:19], 0
  buffer_load_dwordx4 a[156:159], v10, s[8:11], s15 offen offset:3072 sc1
  v_mfma_f32_16x16x32_bf16 v[164:167], a[116:119], a[20:23], v[164:167]
  v_mfma_f32_16x16x32_bf16 v[168:171], a[104:107], a[16:19], 0
  s_add_u32 s69, s61, 2
  v_mfma_f32_16x16x32_bf16 v[168:171], a[120:123], a[20:23], v[168:171]
  v_mfma_f32_16x16x32_bf16 v[172:175], a[108:111], a[16:19], 0
  s_and_b32 s69, s69, 7
  v_mfma_f32_16x16x32_bf16 v[172:175], a[124:127], a[20:23], v[172:175]
  v_mfma_f32_16x16x32_bf16 a[36:39], a[48:51], a[16:19], 0
  ds_read_b128 a[0:3], v49 offset:8192
  s_lshl_b32 s69, s69, 7
  v_mfma_f32_16x16x32_bf16 a[36:39], a[48:51], a[20:23], a[36:39]
  v_mfma_f32_16x16x32_bf16 v[176:179], a[96:99], a[24:27], 0
  ds_read_b128 a[4:7], v50 offset:8192
  s_add_u32 m0, s63, 0
  v_mfma_f32_16x16x32_bf16 v[176:179], a[112:115], a[28:31], v[176:179]
  v_mfma_f32_16x16x32_bf16 v[180:183], a[100:103], a[24:27], 0
  ds_read_b128 a[8:11], v49 offset:10240
  s_add_u32 s70, s65, s69
  v_mfma_f32_16x16x32_bf16 v[180:183], a[116:119], a[28:31], v[180:183]
  v_mfma_f32_16x16x32_bf16 v[184:187], a[104:107], a[24:27], 0
  ds_read_b128 a[12:15], v50 offset:10240
  v_mfma_f32_16x16x32_bf16 v[184:187], a[120:123], a[28:31], v[184:187]
  buffer_load_dwordx4 v51, s[20:23], s70 offen lds
  v_mfma_f32_16x16x32_bf16 v[188:191], a[108:111], a[24:27], 0
  buffer_load_dwordx4 v52, s[20:23], s70 offen offset:1024 lds
  v_mfma_f32_16x16x32_bf16 v[188:191], a[124:127], a[28:31], v[188:191]
  v_mfma_f32_16x16x32_bf16 a[36:39], a[52:55], a[24:27], a[36:39]
  buffer_load_dwordx4 v53, s[20:23], s70 offen offset:2048 lds
  v_mfma_f32_16x16x32_bf16 a[36:39], a[52:55], a[28:31], a[36:39]
  s_waitcnt lgkmcnt(0)
  v_mfma_f32_16x16x32_bf16 v[192:195], a[96:99], a[0:3], 0
  buffer_load_dwordx4 v54, s[20:23], s70 offen offset:3072 lds
  v_mfma_f32_16x16x32_bf16 v[192:195], a[112:115], a[4:7], v[192:195]
  v_mfma_f32_16x16x32_bf16 v[196:199], a[100:103], a[0:3], 0
  s_add_u32 s15, s61, 2
  v_mfma_f32_16x16x32_bf16 v[196:199], a[116:119], a[4:7], v[196:199]
  v_mfma_f32_16x16x32_bf16 v[200:203], a[104:107], a[0:3], 0
  s_and_b32 s15, s15, 7
  v_mfma_f32_16x16x32_bf16 v[200:203], a[120:123], a[4:7], v[200:203]
  v_mfma_f32_16x16x32_bf16 v[204:207], a[108:111], a[0:3], 0
  s_lshl_b32 s15, s15, 13
  v_mfma_f32_16x16x32_bf16 v[204:207], a[124:127], a[4:7], v[204:207]
  v_mfma_f32_16x16x32_bf16 a[40:43], a[48:51], a[0:3], 0
  ds_read_b128 a[16:19], v49 offset:12288
  s_add_u32 s15, s15, s14
  v_mfma_f32_16x16x32_bf16 a[40:43], a[48:51], a[4:7], a[40:43]
  v_mfma_f32_16x16x32_bf16 v[208:211], a[96:99], a[8:11], 0
  ds_read_b128 a[20:23], v50 offset:12288
  v_mfma_f32_16x16x32_bf16 v[208:211], a[112:115], a[12:15], v[208:211]
  buffer_load_dwordx4 a[160:163], v2, s[8:11], s15 offen sc1
  v_mfma_f32_16x16x32_bf16 v[212:215], a[100:103], a[8:11], 0
  ds_read_b128 a[24:27], v49 offset:14336
  v_mfma_f32_16x16x32_bf16 v[212:215], a[116:119], a[12:15], v[212:215]
  buffer_load_dwordx4 a[176:179], v10, s[8:11], s15 offen sc1
  v_mfma_f32_16x16x32_bf16 v[216:219], a[104:107], a[8:11], 0
  ds_read_b128 a[28:31], v50 offset:14336
  v_mfma_f32_16x16x32_bf16 v[216:219], a[120:123], a[12:15], v[216:219]
  buffer_load_dwordx4 a[164:167], v2, s[8:11], s15 offen offset:1024 sc1
  v_mfma_f32_16x16x32_bf16 v[220:223], a[108:111], a[8:11], 0
  buffer_load_dwordx4 a[180:183], v10, s[8:11], s15 offen offset:1024 sc1
  v_mfma_f32_16x16x32_bf16 v[220:223], a[124:127], a[12:15], v[220:223]
  v_mfma_f32_16x16x32_bf16 a[40:43], a[52:55], a[8:11], a[40:43]
  buffer_load_dwordx4 a[168:171], v2, s[8:11], s15 offen offset:2048 sc1
  v_mfma_f32_16x16x32_bf16 a[40:43], a[52:55], a[12:15], a[40:43]
  s_waitcnt lgkmcnt(0)
  v_mfma_f32_16x16x32_bf16 v[224:227], a[96:99], a[16:19], 0
  buffer_load_dwordx4 a[184:187], v10, s[8:11], s15 offen offset:2048 sc1
LS4_%=:
  s_cbranch_execz L2_%=
  v_mfma_f32_16x16x32_bf16 v[224:227], a[112:115], a[20:23], v[224:227]
  v_mfma_f32_16x16x32_bf16 v[228:231], a[100:103], a[16:19], 0
  buffer_load_dwordx4 a[172:175], v2, s[8:11], s15 offen offset:3072 sc1
  v_mfma_f32_16x16x32_bf16 v[228:231], a[116:119], a[20:23], v[228:231]
  s_waitcnt vmcnt(19)
  s_barrier
  buffer_store_dwordx2 v[18:19], v65, s[8:11], s48 offen sc1
  s_mov_b32 s48, 0x9000
  v_mfma_f32_16x16x32_bf16 v[232:235], a[104:107], a[16:19], 0
  v_mfma_f32_16x16x32_bf16 v[232:235], a[120:123], a[20:23], v[232:235]
  buffer_load_dwordx4 a[188:191], v10, s[8:11], s15 offen offset:3072 sc1
  v_mfma_f32_16x16x32_bf16 v[236:239], a[108:111], a[16:19], 0
  v_mfma_f32_16x16x32_bf16 v[236:239], a[124:127], a[20:23], v[236:239]
  v_mfma_f32_16x16x32_bf16 a[44:47], a[48:51], a[16:19], 0
  ds_read_b128 a[0:3], v49 offset:16384
  s_add_u32 m0, s63, 0x1000
  v_mfma_f32_16x16x32_bf16 a[44:47], a[48:51], a[20:23], a[44:47]
  v_mfma_f32_16x16x32_bf16 v[240:243], a[96:99], a[24:27], 0
  ds_read_b128 a[4:7], v50 offset:16384
  v_mfma_f32_16x16x32_bf16 v[240:243], a[112:115], a[28:31], v[240:243]
  s_add_u32 s70, s68, s69
  v_mfma_f32_16x16x32_bf16 v[244:247], a[100:103], a[24:27], 0
  ds_read_b128 a[8:11], v49 offset:18432
  v_mfma_f32_16x16x32_bf16 v[244:247], a[116:119], a[28:31], v[244:247]
  v_mfma_f32_16x16x32_bf16 v[248:251], a[104:107], a[24:27], 0
  ds_read_b128 a[12:15], v50 offset:18432
  v_mfma_f32_16x16x32_bf16 v[248:251], a[120:123], a[28:31], v[248:251]
  buffer_load_dwordx4 v51, s[20:23], s70 offen lds
  v_mfma_f32_16x16x32_bf16 v[252:255], a[108:111], a[24:27], 0
  v_mfma_f32_16x16x32_bf16 v[252:255], a[124:127], a[28:31], v[252:255]
  buffer_load_dwordx4 v52, s[20:23], s70 offen offset:1024 lds
  v_mfma_f32_16x16x32_bf16 a[44:47], a[52:55], a[24:27], a[44:47]
  v_mfma_f32_16x16x32_bf16 a[44:47], a[52:55], a[28:31], a[44:47]
  s_waitcnt lgkmcnt(0)
  s_waitcnt vmcnt(22)
  v_mfma_f32_16x16x32_bf16 v[128:131], a[128:131], a[0:3], v[128:131]
  buffer_load_dwordx4 v53, s[20:23], s70 offen offset:2048 lds
  s_waitcnt vmcnt(22)
  v_mfma_f32_16x16x32_bf16 v[128:131], a[144:147], a[4:7], v[128:131]
  s_waitcnt vmcnt(21)
  v_mfma_f32_16x16x32_bf16 v[132:135], a[132:135], a[0:3], v[132:135]
  buffer_load_dwordx4 v54, s[20:23], s70 offen offset:3072 lds
  s_waitcnt vmcnt(21)
  v_mfma_f32_16x16x32_bf16 v[132:135], a[148:151], a[4:7], v[132:135]
  s_waitcnt vmcnt(20)
  v_mfma_f32_16x16x32_bf16 v[136:139], a[136:139], a[0:3], v[136:139]
  s_waitcnt vmcnt(19)
  v_mfma_f32_16x16x32_bf16 v[136:139], a[152:155], a[4:7], v[136:139]
  s_add_u32 m0, s63, 0x2000
  s_waitcnt vmcnt(18)
  v_mfma_f32_16x16x32_bf16 v[140:143], a[140:143], a[0:3], v[140:143]
  s_waitcnt vmcnt(17)
  v_mfma_f32_16x16x32_bf16 v[140:143], a[156:159], a[4:7], v[140:143]
  s_add_u32 s70, s65, s69
  v_mfma_f32_16x16x32_bf16 a[32:35], a[48:51], a[0:3], a[32:35]
  ds_read_b128 a[16:19], v49 offset:20480
  v_mfma_f32_16x16x32_bf16 a[32:35], a[48:51], a[4:7], a[32:35]
  v_mfma_f32_16x16x32_bf16 v[144:147], a[128:131], a[8:11], v[144:147]
  ds_read_b128 a[20:23], v50 offset:20480
  v_mfma_f32_16x16x32_bf16 v[144:147], a[144:147], a[12:15], v[144:147]
  buffer_load_dwordx4 v51, s[24:27], s70 offen lds
  v_mfma_f32_16x16x32_bf16 v[148:151], a[132:135], a[8:11], v[148:151]
  ds_read_b128 a[24:27], v49 offset:22528
  v_mfma_f32_16x16x32_bf16 v[148:151], a[148:151], a[12:15], v[148:151]
  buffer_load_dwordx4 v52, s[24:27], s70 offen offset:1024 lds
  v_mfma_f32_16x16x32_bf16 v[152:155], a[136:139], a[8:11], v[152:155]
  ds_read_b128 a[28:31], v50 offset:22528
  v_mfma_f32_16x16x32_bf16 v[152:155], a[152:155], a[12:15], v[152:155]
  buffer_load_dwordx4 v53, s[24:27], s70 offen offset:2048 lds
  v_mfma_f32_16x16x32_bf16 v[156:159], a[140:143], a[8:11], v[156:159]
  v_mfma_f32_16x16x32_bf16 v[156:159], a[156:159], a[12:15], v[156:159]
  buffer_load_dwordx4 v54, s[24:27], s70 offen offset:3072 lds
  v_mfma_f32_16x16x32_bf16 a[32:35], a[52:55], a[8:11], a[32:35]
  v_mfma_f32_16x16x32_bf16 a[32:35], a[52:55], a[12:15], a[32:35]
  s_waitcnt lgkmcnt(0)
  v_mfma_f32_16x16x32_bf16 v[160:163], a[128:131], a[16:19], v[160:163]
  s_add_u32 m0, s63, 0x3000
  v_mfma_f32_16x16x32_bf16 v[160:163], a[144:147], a[20:23], v[160:163]
  s_add_u32 s70, s68, s69
  v_mfma_f32_16x16x32_bf16 v[164:167], a[132:135], a[16:19], v[164:167]
  v_mfma_f32_16x16x32_bf16 v[164:167], a[148:151], a[20:23], v[164:167]
  buffer_load_dwordx4 v51, s[24:27], s70 offen lds
  v_mfma_f32_16x16x32_bf16 v[168:171], a[136:139], a[16:19], v[168:171]
  v_mfma_f32_16x16x32_bf16 v[168:171], a[152:155], a[20:23], v[168:171]
  buffer_load_dwordx4 v52, s[24:27], s70 offen offset:1024 lds
  v_mfma_f32_16x16x32_bf16 v[172:175], a[140:143], a[16:19], v[172:175]
  buffer_load_dwordx4 v53, s[24:27], s70 offen offset:2048 lds
  v_mfma_f32_16x16x32_bf16 v[172:175], a[156:159], a[20:23], v[172:175]
  v_mfma_f32_16x16x32_bf16 a[36:39], a[48:51], a[16:19], a[36:39]
  ds_read_b128 a[0:3], v49 offset:24576
  v_mfma_f32_16x16x32_bf16 a[36:39], a[48:51], a[20:23], a[36:39]
  buffer_load_dwordx4 v54, s[24:27], s70 offen offset:3072 lds
  v_mfma_f32_16x16x32_bf16 v[176:179], a[128:131], a[24:27], v[176:179]
  ds_read_b128 a[4:7], v50 offset:24576
  s_add_u32 s69, s61, 3
  v_mfma_f32_16x16x32_bf16 v[176:179], a[144:147], a[28:31], v[176:179]
  s_and_b32 s69, s69, 7
  v_mfma_f32_16x16x32_bf16 v[180:183], a[132:135], a[24:27], v[180:183]
  ds_read_b128 a[8:11], v49 offset:26624
  v_mfma_f32_16x16x32_bf16 v[180:183], a[148:151], a[28:31], v[180:183]
  s_lshl_b32 s69, s69, 7
  v_mfma_f32_16x16x32_bf16 v[184:187], a[136:139], a[24:27], v[184:187]
  ds_read_b128 a[12:15], v50 offset:26624
  v_mfma_f32_16x16x32_bf16 v[184:187], a[152:155], a[28:31], v[184:187]
  s_add_u32 m0, s63, 0x4000
  v_mfma_f32_16x16x32_bf16 v[188:191], a[140:143], a[24:27], v[188:191]
  s_add_u32 s70, s65, s69
  v_mfma_f32_16x16x32_bf16 v[188:191], a[156:159], a[28:31], v[188:191]
  v_mfma_f32_16x16x32_bf16 a[36:39], a[52:55], a[24:27], a[36:39]
  buffer_load_dwordx4 v51, s[20:23], s70 offen lds
  v_mfma_f32_16x16x32_bf16 a[36:39], a[52:55], a[28:31], a[36:39]
  s_waitcnt lgkmcnt(0)
  v_mfma_f32_16x16x32_bf16 v[192:195], a[128:131], a[0:3], v[192:195]
  buffer_load_dwordx4 v52, s[20:23], s70 offen offset:1024 lds
  v_mfma_f32_16x16x32_bf16 v[192:195], a[144:147], a[4:7], v[192:195]
  v_mfma_f32_16x16x32_bf16 v[196:199], a[132:135], a[0:3], v[196:199]
  buffer_load_dwordx4 v53, s[20:23], s70 offen offset:2048 lds
  v_mfma_f32_16x16x32_bf16 v[196:199], a[148:151], a[4:7], v[196:199]
  v_mfma_f32_16x16x32_bf16 v[200:203], a[136:139], a[0:3], v[200:203]
  buffer_load_dwordx4 v54, s[20:23], s70 offen offset:3072 lds
  v_mfma_f32_16x16x32_bf16 v[200:203], a[152:155], a[4:7], v[200:203]
  v_mfma_f32_16x16x32_bf16 v[204:207], a[140:143], a[0:3], v[204:207]
  s_add_u32 s15, s61, 3
  v_mfma_f32_16x16x32_bf16 v[204:207], a[156:159], a[4:7], v[204:207]
  v_mfma_f32_16x16x32_bf16 a[40:43], a[48:51], a[0:3], a[40:43]
  ds_read_b128 a[16:19], v49 offset:28672
  s_and_b32 s15, s15, 7
  v_mfma_f32_16x16x32_bf16 a[40:43], a[48:51], a[4:7], a[40:43]
  v_mfma_f32_16x16x32_bf16 v[208:211], a[128:131], a[8:11], v[208:211]
  ds_read_b128 a[20:23], v50 offset:28672
  s_lshl_b32 s15, s15, 13
  v_mfma_f32_16x16x32_bf16 v[208:211], a[144:147], a[12:15], v[208:211]
  v_mfma_f32_16x16x32_bf16 v[212:215], a[132:135], a[8:11], v[212:215]
  ds_read_b128 a[24:27], v49 offset:30720
  s_add_u32 s15, s15, s14
  v_mfma_f32_16x16x32_bf16 v[212:215], a[148:151], a[12:15], v[212:215]
  v_mfma_f32_16x16x32_bf16 v[216:219], a[136:139], a[8:11], v[216:219]
  ds_read_b128 a[28:31], v50 offset:30720
  v_mfma_f32_16x16x32_bf16 v[216:219], a[152:155], a[12:15], v[216:219]
  buffer_load_dwordx4 a[96:99], v2, s[8:11], s15 offen sc1
LS5_%=:
  s_cbranch_execz L2_%=
  v_mfma_f32_16x16x32_bf16 v[220:223], a[140:143], a[8:11], v[220:223]
  buffer_load_dwordx4 a[112:115], v10, s[8:11], s15 offen sc1
  v_mfma_f32_16x16x32_bf16 v[220:223], a[156:159], a[12:15], v[220:223]
  v_mfma_f32_16x16x32_bf16 a[40:43], a[52:55], a[8:11], a[40:43]
  buffer_load_dwordx4 a[100:103], v2, s[8:11], s15 offen offset:1024 sc1
  v_mfma_f32_16x16x32_bf16 a[40:43], a[52:55], a[12:15], a[40:43]
  s_waitcnt lgkmcnt(0)
  v_mfma_f32_16x16x32_bf16 v[224:227], a[128:131], a[16:19], v[224:227]
  buffer_load_dwordx4 a[116:119], v10, s[8:11], s15 offen offset:1024 sc1
  v_mfma_f32_16x16x32_bf16 v[224:227], a[144:147], a[20:23], v[224:227]
  v_mfma_f32_16x16x32_bf16 v[228:231], a[132:135], a[16:19], v[228:231]
  v_mfma_f32_16x16x32_bf16 v[228:231], a[148:151], a[20:23], v[228:231]
  v_mfma_f32_16x16x32_bf16 v[232:235], a[136:139], a[16:19], v[232:235]
  buffer_load_dwordx4 a[104:107], v2, s[8:11], s15 offen offset:2048 sc1
  v_mfma_f32_16x16x32_bf16 v[232:235], a[152:155], a[20:23], v[232:235]
  v_mfma_f32_16x16x32_bf16 v[236:239], a[140:143], a[16:19], v[236:239]
  v_mfma_f32_16x16x32_bf16 v[236:239], a[156:159], a[20:23], v[236:239]
  v_mfma_f32_16x16x32_bf16 a[44:47], a[48:51], a[16:19], a[44:47]
  s_waitcnt vmcnt(30)
  ds_read_b128 a[0:3], v49
  v_mfma_f32_16x16x32_bf16 a[44:47], a[48:51], a[20:23], a[44:47]
  buffer_load_dwordx4 a[120:123], v10, s[8:11], s15 offen offset:2048 sc1
  v_mfma_f32_16x16x32_bf16 v[240:243], a[128:131], a[24:27], v[240:243]
  ds_read_b128 a[4:7], v50
  v_mfma_f32_16x16x32_bf16 v[240:243], a[144:147], a[28:31], v[240:243]
  v_mfma_f32_16x16x32_bf16 v[244:247], a[132:135], a[24:27], v[244:247]
  ds_read_b128 a[8:11], v49 offset:2048
  v_mfma_f32_16x16x32_bf16 v[244:247], a[148:151], a[28:31], v[244:247]
  buffer_load_dwordx4 a[108:111], v2, s[8:11], s15 offen offset:3072 sc1
  v_mfma_f32_16x16x32_bf16 v[248:251], a[136:139], a[24:27], v[248:251]
  ds_read_b128 a[12:15], v50 offset:2048
  v_mfma_f32_16x16x32_bf16 v[248:251], a[152:155], a[28:31], v[248:251]
  v_mfma_f32_16x16x32_bf16 v[252:255], a[140:143], a[24:27], v[252:255]
  buffer_load_dwordx4 a[124:127], v10, s[8:11], s15 offen offset:3072 sc1
  v_mfma_f32_16x16x32_bf16 v[252:255], a[156:159], a[28:31], v[252:255]
  v_mfma_f32_16x16x32_bf16 a[44:47], a[52:55], a[24:27], a[44:47]
  v_mfma_f32_16x16x32_bf16 a[44:47], a[52:55], a[28:31], a[44:47]
  s_waitcnt lgkmcnt(0)
  s_waitcnt vmcnt(32)
  v_mfma_f32_16x16x32_bf16 v[128:131], a[160:163], a[0:3], v[128:131]
  s_add_u32 m0, s63, 0x5000
  s_waitcnt vmcnt(31)
  v_mfma_f32_16x16x32_bf16 v[128:131], a[176:179], a[4:7], v[128:131]
  s_waitcnt vmcnt(30)
  v_mfma_f32_16x16x32_bf16 v[132:135], a[164:167], a[0:3], v[132:135]
  s_add_u32 s70, s68, s69
  s_waitcnt vmcnt(29)
  v_mfma_f32_16x16x32_bf16 v[132:135], a[180:183], a[4:7], v[132:135]
  s_waitcnt vmcnt(28)
  v_mfma_f32_16x16x32_bf16 v[136:139], a[168:171], a[0:3], v[136:139]
  buffer_load_dwordx4 v51, s[20:23], s70 offen lds
  s_waitcnt vmcnt(28)
  v_mfma_f32_16x16x32_bf16 v[136:139], a[184:187], a[4:7], v[136:139]
  s_waitcnt vmcnt(27)
  v_mfma_f32_16x16x32_bf16 v[140:143], a[172:175], a[0:3], v[140:143]
  buffer_load_dwordx4 v52, s[20:23], s70 offen offset:1024 lds
  s_waitcnt vmcnt(26)
  v_mfma_f32_16x16x32_bf16 v[140:143], a[188:191], a[4:7], v[140:143]
  v_mfma_f32_16x16x32_bf16 a[32:35], a[48:51], a[0:3], a[32:35]
  s_waitcnt vmcnt(22)
  ds_read_b128 a[16:19], v49 offset:4096
  v_mfma_f32_16x16x32_bf16 a[32:35], a[48:51], a[4:7], a[32:35]
  buffer_load_dwordx4 v53, s[20:23], s70 offen offset:2048 lds
  v_mfma_f32_16x16x32_bf16 v[144:147], a[160:163], a[8:11], v[144:147]
  ds_read_b128 a[20:23], v50 offset:4096
  v_mfma_f32_16x16x32_bf16 v[144:147], a[176:179], a[12:15], v[144:147]
  buffer_load_dwordx4 v54, s[20:23], s70 offen offset:3072 lds
  v_mfma_f32_16x16x32_bf16 v[148:151], a[164:167], a[8:11], v[148:151]
  ds_read_b128 a[24:27], v49 offset:6144
  s_add_u32 m0, s63, 0x6000
  v_mfma_f32_16x16x32_bf16 v[148:151], a[180:183], a[12:15], v[148:151]
  v_mfma_f32_16x16x32_bf16 v[152:155], a[168:171], a[8:11], v[152:155]
  ds_read_b128 a[28:31], v50 offset:6144
  s_add_u32 s70, s65, s69
  v_mfma_f32_16x16x32_bf16 v[152:155], a[184:187], a[12:15], v[152:155]
  v_mfma_f32_16x16x32_bf16 v[156:159], a[172:175], a[8:11], v[156:159]
  buffer_load_dwordx4 v51, s[24:27], s70 offen lds
  v_mfma_f32_16x16x32_bf16 v[156:159], a[188:191], a[12:15], v[156:159]
  v_mfma_f32_16x16x32_bf16 a[32:35], a[52:55], a[8:11], a[32:35]
  buffer_load_dwordx4 v52, s[24:27], s70 offen offset:1024 lds
  v_mfma_f32_16x16x32_bf16 a[32:35], a[52:55], a[12:15], a[32:35]
  s_waitcnt lgkmcnt(0)
  v_mfma_f32_16x16x32_bf16 v[160:163], a[160:163], a[16:19], v[160:163]
  buffer_load_dwordx4 v53, s[24:27], s70 offen offset:2048 lds
  v_mfma_f32_16x16x32_bf16 v[160:163], a[176:179], a[20:23], v[160:163]
  v_mfma_f32_16x16x32_bf16 v[164:167], a[164:167], a[16:19], v[164:167]
  buffer_load_dwordx4 v54, s[24:27], s70 offen offset:3072 lds
  v_mfma_f32_16x16x32_bf16 v[164:167], a[180:183], a[20:23], v[164:167]
  v_mfma_f32_16x16x32_bf16 v[168:171], a[168:171], a[16:19], v[168:171]
  v_mfma_f32_16x16x32_bf16 v[168:171], a[184:187], a[20:23], v[168:171]
  s_add_u32 m0, s63, 0x7000
  v_mfma_f32_16x16x32_bf16 v[172:175], a[172:175], a[16:19], v[172:175]
  v_mfma_f32_16x16x32_bf16 v[172:175], a[188:191], a[20:23], v[172:175]
  v_mfma_f32_16x16x32_bf16 a[36:39], a[48:51], a[16:19], a[36:39]
  s_waitcnt vmcnt(24)
  ds_read_b128 a[0:3], v49 offset:8192
  s_add_u32 s70, s68, s69
  v_mfma_f32_16x16x32_bf16 a[36:39], a[48:51], a[20:23], a[36:39]
  v_mfma_f32_16x16x32_bf16 v[176:179], a[160:163], a[24:27], v[176:179]
  ds_read_b128 a[4:7], v50 offset:8192
  v_mfma_f32_16x16x32_bf16 v[176:179], a[176:179], a[28:31], v[176:179]
  buffer_load_dwordx4 v51, s[24:27], s70 offen lds
  v_mfma_f32_16x16x32_bf16 v[180:183], a[164:167], a[24:27], v[180:183]
  ds_read_b128 a[8:11], v49 offset:10240
  v_mfma_f32_16x16x32_bf16 v[180:183], a[180:183], a[28:31], v[180:183]
  v_mfma_f32_16x16x32_bf16 v[184:187], a[168:171], a[24:27], v[184:187]
  ds_read_b128 a[12:15], v50 offset:10240
  v_mfma_f32_16x16x32_bf16 v[184:187], a[184:187], a[28:31], v[184:187]
  buffer_load_dwordx4 v52, s[24:27], s70 offen offset:1024 lds
  v_mfma_f32_16x16x32_bf16 v[188:191], a[172:175], a[24:27], v[188:191]
  v_mfma_f32_16x16x32_bf16 v[188:191], a[188:191], a[28:31], v[188:191]
  buffer_load_dwordx4 v53, s[24:27], s70 offen offset:2048 lds
  v_mfma_f32_16x16x32_bf16 a[36:39], a[52:55], a[24:27], a[36:39]
  v_mfma_f32_16x16x32_bf16 a[36:39], a[52:55], a[28:31], a[36:39]
  s_waitcnt lgkmcnt(0)
  v_mfma_f32_16x16x32_bf16 v[192:195], a[160:163], a[0:3], v[192:195]
  buffer_load_dwordx4 v54, s[24:27], s70 offen offset:3072 lds
  v_mfma_f32_16x16x32_bf16 v[192:195], a[176:179], a[4:7], v[192:195]
  s_add_u32 s69, s61, 4
  v_mfma_f32_16x16x32_bf16 v[196:199], a[164:167], a[0:3], v[196:199]
  s_and_b32 s69, s69, 7
  v_mfma_f32_16x16x32_bf16 v[196:199], a[180:183], a[4:7], v[196:199]
  v_mfma_f32_16x16x32_bf16 v[200:203], a[168:171], a[0:3], v[200:203]
  s_lshl_b32 s69, s69, 7
  v_mfma_f32_16x16x32_bf16 v[200:203], a[184:187], a[4:7], v[200:203]
  s_add_u32 m0, s63, 0
  v_mfma_f32_16x16x32_bf16 v[204:207], a[172:175], a[0:3], v[204:207]
  s_add_u32 s70, s65, s69
  v_mfma_f32_16x16x32_bf16 v[204:207], a[188:191], a[4:7], v[204:207]
  v_mfma_f32_16x16x32_bf16 a[40:43], a[48:51], a[0:3], a[40:43]
  s_waitcnt vmcnt(24)
  ds_read_b128 a[16:19], v49 offset:12288
  v_mfma_f32_16x16x32_bf16 a[40:43], a[48:51], a[4:7], a[40:43]
  buffer_load_dwordx4 v51, s[20:23], s70 offen lds
  v_mfma_f32_16x16x32_bf16 v[208:211], a[160:163], a[8:11], v[208:211]
  ds_read_b128 a[20:23], v50 offset:12288
  v_mfma_f32_16x16x32_bf16 v[208:211], a[176:179], a[12:15], v[208:211]
  buffer_load_dwordx4 v52, s[20:23], s70 offen offset:1024 lds
  v_mfma_f32_16x16x32_bf16 v[212:215], a[164:167], a[8:11], v[212:215]
LS6_%=:
  s_cbranch_execz L2_%=
  ds_read_b128 a[24:27], v49 offset:14336
  v_mfma_f32_16x16x32_bf16 v[212:215], a[180:183], a[12:15], v[212:215]
  buffer_load_dwordx4 v53, s[20:23], s70 offen offset:2048 lds
  v_mfma_f32_16x16x32_bf16 v[216:219], a[168:171], a[8:11], v[216:219]
  ds_read_b128 a[28:31], v50 offset:14336
  v_mfma_f32_16x16x32_bf16 v[216:219], a[184:187], a[12:15], v[216:219]
  buffer_load_dwordx4 v54, s[20:23], s70 offen offset:3072 lds
  v_mfma_f32_16x16x32_bf16 v[220:223], a[172:175], a[8:11], v[220:223]
  s_add_u32 s15, s61, 4
  s_and_b32 s15, s15, 7
  s_lshl_b32 s15, s15, 13
  v_mfma_f32_16x16x32_bf16 v[220:223], a[188:191], a[12:15], v[220:223]
  s_add_u32 s15, s15, s14
  v_mfma_f32_16x16x32_bf16 a[40:43], a[52:55], a[8:11], a[40:43]
  buffer_load_dwordx4 a[128:131], v2, s[8:11], s15 offen sc1
  v_mfma_f32_16x16x32_bf16 a[40:43], a[52:55], a[12:15], a[40:43]
  s_waitcnt lgkmcnt(0)
  v_mfma_f32_16x16x32_bf16 v[224:227], a[160:163], a[16:19], v[224:227]
  buffer_load_dwordx4 a[144:147], v10, s[8:11], s15 offen sc1
  v_mfma_f32_16x16x32_bf16 v[224:227], a[176:179], a[20:23], v[224:227]
  v_mfma_f32_16x16x32_bf16 v[228:231], a[164:167], a[16:19], v[228:231]
  v_mfma_f32_16x16x32_bf16 v[228:231], a[180:183], a[20:23], v[228:231]
  v_mfma_f32_16x16x32_bf16 v[232:235], a[168:171], a[16:19], v[232:235]
  buffer_load_dwordx4 a[132:135], v2, s[8:11], s15 offen offset:1024 sc1
  v_mfma_f32_16x16x32_bf16 v[232:235], a[184:187], a[20:23], v[232:235]
  v_mfma_f32_16x16x32_bf16 v[236:239], a[172:175], a[16:19], v[236:239]
  v_mfma_f32_16x16x32_bf16 v[236:239], a[188:191], a[20:23], v[236:239]
  v_mfma_f32_16x16x32_bf16 a[44:47], a[48:51], a[16:19], a[44:47]
  s_waitcnt vmcnt(27)
  ds_read_b128 a[0:3], v49 offset:16384
  v_mfma_f32_16x16x32_bf16 a[44:47], a[48:51], a[20:23], a[44:47]
  buffer_load_dwordx4 a[148:151], v10, s[8:11], s15 offen offset:1024 sc1
  v_mfma_f32_16x16x32_bf16 v[240:243], a[160:163], a[24:27], v[240:243]
  ds_read_b128 a[4:7], v50 offset:16384
  v_mfma_f32_16x16x32_bf16 v[240:243], a[176:179], a[28:31], v[240:243]
  v_mfma_f32_16x16x32_bf16 v[244:247], a[164:167], a[24:27], v[244:247]
  ds_read_b128 a[8:11], v49 offset:18432
  v_mfma_f32_16x16x32_bf16 v[244:247], a[180:183], a[28:31], v[244:247]
  buffer_load_dwordx4 a[136:139], v2, s[8:11], s15 offen offset:2048 sc1
  v_mfma_f32_16x16x32_bf16 v[248:251], a[168:171], a[24:27], v[248:251]
  ds_read_b128 a[12:15], v50 offset:18432
  v_mfma_f32_16x16x32_bf16 v[248:251], a[184:187], a[28:31], v[248:251]
  v_mfma_f32_16x16x32_bf16 v[252:255], a[172:175], a[24:27], v[252:255]
  buffer_load_dwordx4 a[152:155], v10, s[8:11], s15 offen offset:2048 sc1
  v_mfma_f32_16x16x32_bf16 v[252:255], a[188:191], a[28:31], v[252:255]
  v_mfma_f32_16x16x32_bf16 a[44:47], a[52:55], a[24:27], a[44:47]
  v_mfma_f32_16x16x32_bf16 a[44:47], a[52:55], a[28:31], a[44:47]
  s_waitcnt lgkmcnt(0)
  s_waitcnt vmcnt(29)
  v_mfma_f32_16x16x32_bf16 v[128:131], a[96:99], a[0:3], v[128:131]
  buffer_load_dwordx4 a[140:143], v2, s[8:11], s15 offen offset:3072 sc1
  s_waitcnt vmcnt(29)
  v_mfma_f32_16x16x32_bf16 v[128:131], a[112:115], a[4:7], v[128:131]
  s_waitcnt vmcnt(28)
  v_mfma_f32_16x16x32_bf16 v[132:135], a[100:103], a[0:3], v[132:135]
  buffer_load_dwordx4 a[156:159], v10, s[8:11], s15 offen offset:3072 sc1
  s_waitcnt vmcnt(28)
  v_mfma_f32_16x16x32_bf16 v[132:135], a[116:119], a[4:7], v[132:135]
  s_waitcnt vmcnt(27)
  v_mfma_f32_16x16x32_bf16 v[136:139], a[104:107], a[0:3], v[136:139]
  s_waitcnt vmcnt(26)
  v_mfma_f32_16x16x32_bf16 v[136:139], a[120:123], a[4:7], v[136:139]
  s_add_u32 m0, s63, 0x1000
  s_waitcnt vmcnt(25)
  v_mfma_f32_16x16x32_bf16 v[140:143], a[108:111], a[0:3], v[140:143]
  s_waitcnt vmcnt(24)
  v_mfma_f32_16x16x32_bf16 v[140:143], a[124:127], a[4:7], v[140:143]
  s_add_u32 s70, s68, s69
  v_mfma_f32_16x16x32_bf16 a[32:35], a[48:51], a[0:3], a[32:35]
  s_waitcnt vmcnt(20)
  ds_read_b128 a[16:19], v49 offset:20480
  v_mfma_f32_16x16x32_bf16 a[32:35], a[48:51], a[4:7], a[32:35]
  v_mfma_f32_16x16x32_bf16 v[144:147], a[96:99], a[8:11], v[144:147]
  ds_read_b128 a[20:23], v50 offset:20480
  v_mfma_f32_16x16x32_bf16 v[144:147], a[112:115], a[12:15], v[144:147]
  buffer_load_dwordx4 v51, s[20:23], s70 offen lds
  v_mfma_f32_16x16x32_bf16 v[148:151], a[100:103], a[8:11], v[148:151]
  ds_read_b128 a[24:27], v49 offset:22528
  v_mfma_f32_16x16x32_bf16 v[148:151], a[116:119], a[12:15], v[148:151]
  buffer_load_dwordx4 v52, s[20:23], s70 offen offset:1024 lds
  v_mfma_f32_16x16x32_bf16 v[152:155], a[104:107], a[8:11], v[152:155]
  ds_read_b128 a[28:31], v50 offset:22528
  v_mfma_f32_16x16x32_bf16 v[152:155], a[120:123], a[12:15], v[152:155]
  buffer_load_dwordx4 v53, s[20:23], s70 offen offset:2048 lds
  v_mfma_f32_16x16x32_bf16 v[156:159], a[108:111], a[8:11], v[156:159]
  v_mfma_f32_16x16x32_bf16 v[156:159], a[124:127], a[12:15], v[156:159]
  buffer_load_dwordx4 v54, s[20:23], s70 offen offset:3072 lds
  v_mfma_f32_16x16x32_bf16 a[32:35], a[52:55], a[8:11], a[32:35]
  v_mfma_f32_16x16x32_bf16 a[32:35], a[52:55], a[12:15], a[32:35]
  s_waitcnt lgkmcnt(0)
  v_mfma_f32_16x16x32_bf16 v[160:163], a[96:99], a[16:19], v[160:163]
  s_add_u32 m0, s63, 0x2000
  v_mfma_f32_16x16x32_bf16 v[160:163], a[112:115], a[20:23], v[160:163]
  v_mfma_f32_16x16x32_bf16 v[164:167], a[100:103], a[16:19], v[164:167]
  s_add_u32 s70, s65, s69
  v_mfma_f32_16x16x32_bf16 v[164:167], a[116:119], a[20:23], v[164:167]
  v_mfma_f32_16x16x32_bf16 v[168:171], a[104:107], a[16:19], v[168:171]
  buffer_load_dwordx4 v51, s[24:27], s70 offen lds
  v_mfma_f32_16x16x32_bf16 v[168:171], a[120:123], a[20:23], v[168:171]
  v_mfma_f32_16x16x32_bf16 v[172:175], a[108:111], a[16:19], v[172:175]
  buffer_load_dwordx4 v52, s[24:27], s70 offen offset:1024 lds
  v_mfma_f32_16x16x32_bf16 v[172:175], a[124:127], a[20:23], v[172:175]
  v_mfma_f32_16x16x32_bf16 a[36:39], a[48:51], a[16:19], a[36:39]
  s_waitcnt vmcnt(22)
  ds_read_b128 a[0:3], v49 offset:24576
  v_mfma_f32_16x16x32_bf16 a[36:39], a[48:51], a[20:23], a[36:39]
  buffer_load_dwordx4 v53, s[24:27], s70 offen offset:2048 lds
  v_mfma_f32_16x16x32_bf16 v[176:179], a[96:99], a[24:27], v[176:179]
  ds_read_b128 a[4:7], v50 offset:24576
  v_mfma_f32_16x16x32_bf16 v[176:179], a[112:115], a[28:31], v[176:179]
  buffer_load_dwordx4 v54, s[24:27], s70 offen offset:3072 lds
  v_mfma_f32_16x16x32_bf16 v[180:183], a[100:103], a[24:27], v[180:183]
  ds_read_b128 a[8:11], v49 offset:26624
  v_mfma_f32_16x16x32_bf16 v[180:183], a[116:119], a[28:31], v[180:183]
  s_add_u32 m0, s63, 0x3000
  v_mfma_f32_16x16x32_bf16 v[184:187], a[104:107], a[24:27], v[184:187]
  ds_read_b128 a[12:15], v50 offset:26624
  v_mfma_f32_16x16x32_bf16 v[184:187], a[120:123], a[28:31], v[184:187]
  s_add_u32 s70, s68, s69
  v_mfma_f32_16x16x32_bf16 v[188:191], a[108:111], a[24:27], v[188:191]
  v_mfma_f32_16x16x32_bf16 v[188:191], a[124:127], a[28:31], v[188:191]
  buffer_load_dwordx4 v51, s[24:27], s70 offen lds
  v_mfma_f32_16x16x32_bf16 a[36:39], a[52:55], a[24:27], a[36:39]
  v_mfma_f32_16x16x32_bf16 a[36:39], a[52:55], a[28:31], a[36:39]
  s_waitcnt lgkmcnt(0)
  v_mfma_f32_16x16x32_bf16 v[192:195], a[96:99], a[0:3], v[192:195]
  buffer_load_dwordx4 v52, s[24:27], s70 offen offset:1024 lds
  v_mfma_f32_16x16x32_bf16 v[192:195], a[112:115], a[4:7], v[192:195]
  buffer_load_dwordx4 v53, s[24:27], s70 offen offset:2048 lds
  v_mfma_f32_16x16x32_bf16 v[196:199], a[100:103], a[0:3], v[196:199]
  v_mfma_f32_16x16x32_bf16 v[196:199], a[116:119], a[4:7], v[196:199]
  buffer_load_dwordx4 v54, s[24:27], s70 offen offset:3072 lds
  v_mfma_f32_16x16x32_bf16 v[200:203], a[104:107], a[0:3], v[200:203]
  v_mfma_f32_16x16x32_bf16 v[200:203], a[120:123], a[4:7], v[200:203]
  s_add_u32 s69, s61, 5
  v_mfma_f32_16x16x32_bf16 v[204:207], a[108:111], a[0:3], v[204:207]
  v_mfma_f32_16x16x32_bf16 v[204:207], a[124:127], a[4:7], v[204:207]
  s_and_b32 s69, s69, 7
  v_mfma_f32_16x16x32_bf16 a[40:43], a[48:51], a[0:3], a[40:43]
  s_waitcnt vmcnt(24)
LS7_%=:
  s_cbranch_execz L2_%=
  ds_read_b128 a[16:19], v49 offset:28672
  v_mfma_f32_16x16x32_bf16 a[40:43], a[48:51], a[4:7], a[40:43]
  s_lshl_b32 s69, s69, 7
  v_mfma_f32_16x16x32_bf16 v[208:211], a[96:99], a[8:11], v[208:211]
  ds_read_b128 a[20:23], v50 offset:28672
  s_add_u32 m0, s63, 0x4000
  v_mfma_f32_16x16x32_bf16 v[208:211], a[112:115], a[12:15], v[208:211]
  v_mfma_f32_16x16x32_bf16 v[212:215], a[100:103], a[8:11], v[212:215]
  ds_read_b128 a[24:27], v49 offset:30720
  s_add_u32 s70, s65, s69
  v_mfma_f32_16x16x32_bf16 v[212:215], a[116:119], a[12:15], v[212:215]
  v_mfma_f32_16x16x32_bf16 v[216:219], a[104:107], a[8:11], v[216:219]
  ds_read_b128 a[28:31], v50 offset:30720
  v_mfma_f32_16x16x32_bf16 v[216:219], a[120:123], a[12:15], v[216:219]
  buffer_load_dwordx4 v51, s[20:23], s70 offen lds
  v_mfma_f32_16x16x32_bf16 v[220:223], a[108:111], a[8:11], v[220:223]
  buffer_load_dwordx4 v52, s[20:23], s70 offen offset:1024 lds
  v_mfma_f32_16x16x32_bf16 v[220:223], a[124:127], a[12:15], v[220:223]
  v_mfma_f32_16x16x32_bf16 a[40:43], a[52:55], a[8:11], a[40:43]
  buffer_load_dwordx4 v53, s[20:23], s70 offen offset:2048 lds
  v_mfma_f32_16x16x32_bf16 a[40:43], a[52:55], a[12:15], a[40:43]
  s_waitcnt lgkmcnt(0)
  v_mfma_f32_16x16x32_bf16 v[224:227], a[96:99], a[16:19], v[224:227]
  buffer_load_dwordx4 v54, s[20:23], s70 offen offset:3072 lds
  v_mfma_f32_16x16x32_bf16 v[224:227], a[112:115], a[20:23], v[224:227]
  v_mfma_f32_16x16x32_bf16 v[228:231], a[100:103], a[16:19], v[228:231]
  s_add_u32 s15, s61, 5
  v_mfma_f32_16x16x32_bf16 v[228:231], a[116:119], a[20:23], v[228:231]
  v_mfma_f32_16x16x32_bf16 v[232:235], a[104:107], a[16:19], v[232:235]
  s_and_b32 s15, s15, 7
  v_mfma_f32_16x16x32_bf16 v[232:235], a[120:123], a[20:23], v[232:235]
  v_mfma_f32_16x16x32_bf16 v[236:239], a[108:111], a[16:19], v[236:239]
  s_lshl_b32 s15, s15, 13
  v_mfma_f32_16x16x32_bf16 v[236:239], a[124:127], a[20:23], v[236:239]
  v_mfma_f32_16x16x32_bf16 a[44:47], a[48:51], a[16:19], a[44:47]
  s_waitcnt vmcnt(24)
  ds_read_b128 a[0:3], v49
  s_add_u32 s15, s15, s14
  v_mfma_f32_16x16x32_bf16 a[44:47], a[48:51], a[20:23], a[44:47]
  v_mfma_f32_16x16x32_bf16 v[240:243], a[96:99], a[24:27], v[240:243]
  ds_read_b128 a[4:7], v50
  v_mfma_f32_16x16x32_bf16 v[240:243], a[112:115], a[28:31], v[240:243]
  buffer_load_dwordx4 a[160:163], v2, s[8:11], s15 offen sc1
  v_mfma_f32_16x16x32_bf16 v[244:247], a[100:103], a[24:27], v[244:247]
  ds_read_b128 a[8:11], v49 offset:2048
  v_mfma_f32_16x16x32_bf16 v[244:247], a[116:119], a[28:31], v[244:247]
  buffer_load_dwordx4 a[176:179], v10, s[8:11], s15 offen sc1
  v_mfma_f32_16x16x32_bf16 v[248:251], a[104:107], a[24:27], v[248:251]
  ds_read_b128 a[12:15], v50 offset:2048
  v_mfma_f32_16x16x32_bf16 v[248:251], a[120:123], a[28:31], v[248:251]
  buffer_load_dwordx4 a[164:167], v2, s[8:11], s15 offen offset:1024 sc1
  v_mfma_f32_16x16x32_bf16 v[252:255], a[108:111], a[24:27], v[252:255]
  v_mfma_f32_16x16x32_bf16 v[252:255], a[124:127], a[28:31], v[252:255]
  buffer_load_dwordx4 a[180:183], v10, s[8:11], s15 offen offset:1024 sc1
  v_mfma_f32_16x16x32_bf16 a[44:47], a[52:55], a[24:27], a[44:47]
  v_mfma_f32_16x16x32_bf16 a[44:47], a[52:55], a[28:31], a[44:47]
  s_waitcnt lgkmcnt(0)
  s_waitcnt vmcnt(27)
  v_mfma_f32_16x16x32_bf16 v[128:131], a[128:131], a[0:3], v[128:131]
  buffer_load_dwordx4 a[168:171], v2, s[8:11], s15 offen offset:2048 sc1
  s_waitcnt vmcnt(27)
  v_mfma_f32_16x16x32_bf16 v[128:131], a[144:147], a[4:7], v[128:131]
  s_waitcnt vmcnt(26)
  v_mfma_f32_16x16x32_bf16 v[132:135], a[132:135], a[0:3], v[132:135]
  buffer_load_dwordx4 a[184:187], v10, s[8:11], s15 offen offset:2048 sc1
  s_waitcnt vmcnt(26)
  v_mfma_f32_16x16x32_bf16 v[132:135], a[148:151], a[4:7], v[132:135]
  s_waitcnt vmcnt(25)
  v_mfma_f32_16x16x32_bf16 v[136:139], a[136:139], a[0:3], v[136:139]
  s_waitcnt vmcnt(24)
  v_mfma_f32_16x16x32_bf16 v[136:139], a[152:155], a[4:7], v[136:139]
  buffer_load_dwordx4 a[172:175], v2, s[8:11], s15 offen offset:3072 sc1
  s_waitcnt vmcnt(24)
  v_mfma_f32_16x16x32_bf16 v[140:143], a[140:143], a[0:3], v[140:143]
  s_waitcnt vmcnt(23)
  v_mfma_f32_16x16x32_bf16 v[140:143], a[156:159], a[4:7], v[140:143]
  buffer_load_dwordx4 a[188:191], v10, s[8:11], s15 offen offset:3072 sc1
  v_mfma_f32_16x16x32_bf16 a[32:35], a[48:51], a[0:3], a[32:35]
  s_waitcnt vmcnt(20)
  ds_read_b128 a[16:19], v49 offset:4096
  v_mfma_f32_16x16x32_bf16 a[32:35], a[48:51], a[4:7], a[32:35]
  v_mfma_f32_16x16x32_bf16 v[144:147], a[128:131], a[8:11], v[144:147]
  ds_read_b128 a[20:23], v50 offset:4096
  s_add_u32 m0, s63, 0x5000
  v_mfma_f32_16x16x32_bf16 v[144:147], a[144:147], a[12:15], v[144:147]
  v_mfma_f32_16x16x32_bf16 v[148:151], a[132:135], a[8:11], v[148:151]
  ds_read_b128 a[24:27], v49 offset:6144
  s_add_u32 s70, s68, s69
  v_mfma_f32_16x16x32_bf16 v[148:151], a[148:151], a[12:15], v[148:151]
  v_mfma_f32_16x16x32_bf16 v[152:155], a[136:139], a[8:11], v[152:155]
  ds_read_b128 a[28:31], v50 offset:6144
  v_mfma_f32_16x16x32_bf16 v[152:155], a[152:155], a[12:15], v[152:155]
  buffer_load_dwordx4 v51, s[20:23], s70 offen lds
  v_mfma_f32_16x16x32_bf16 v[156:159], a[140:143], a[8:11], v[156:159]
  v_mfma_f32_16x16x32_bf16 v[156:159], a[156:159], a[12:15], v[156:159]
  buffer_load_dwordx4 v52, s[20:23], s70 offen offset:1024 lds
  v_mfma_f32_16x16x32_bf16 a[32:35], a[52:55], a[8:11], a[32:35]
  v_mfma_f32_16x16x32_bf16 a[32:35], a[52:55], a[12:15], a[32:35]
  s_waitcnt lgkmcnt(0)
  v_mfma_f32_16x16x32_bf16 v[160:163], a[128:131], a[16:19], v[160:163]
  buffer_load_dwordx4 v53, s[20:23], s70 offen offset:2048 lds
  v_mfma_f32_16x16x32_bf16 v[160:163], a[144:147], a[20:23], v[160:163]
  v_mfma_f32_16x16x32_bf16 v[164:167], a[132:135], a[16:19], v[164:167]
  buffer_load_dwordx4 v54, s[20:23], s70 offen offset:3072 lds
  v_mfma_f32_16x16x32_bf16 v[164:167], a[148:151], a[20:23], v[164:167]
  v_mfma_f32_16x16x32_bf16 v[168:171], a[136:139], a[16:19], v[168:171]
  v_mfma_f32_16x16x32_bf16 v[168:171], a[152:155], a[20:23], v[168:171]
  s_add_u32 m0, s63, 0x6000
  v_mfma_f32_16x16x32_bf16 v[172:175], a[140:143], a[16:19], v[172:175]
  v_mfma_f32_16x16x32_bf16 v[172:175], a[156:159], a[20:23], v[172:175]
  v_mfma_f32_16x16x32_bf16 a[36:39], a[48:51], a[16:19], a[36:39]
  s_waitcnt vmcnt(20)
  ds_read_b128 a[0:3], v49 offset:8192
  s_add_u32 s70, s65, s69
  v_mfma_f32_16x16x32_bf16 a[36:39], a[48:51], a[20:23], a[36:39]
  v_mfma_f32_16x16x32_bf16 v[176:179], a[128:131], a[24:27], v[176:179]
  ds_read_b128 a[4:7], v50 offset:8192
  v_mfma_f32_16x16x32_bf16 v[176:179], a[144:147], a[28:31], v[176:179]
  buffer_load_dwordx4 v51, s[24:27], s70 offen lds
  v_mfma_f32_16x16x32_bf16 v[180:183], a[132:135], a[24:27], v[180:183]
  ds_read_b128 a[8:11], v49 offset:10240
  v_mfma_f32_16x16x32_bf16 v[180:183], a[148:151], a[28:31], v[180:183]
  v_mfma_f32_16x16x32_bf16 v[184:187], a[136:139], a[24:27], v[184:187]
  ds_read_b128 a[12:15], v50 offset:10240
  v_mfma_f32_16x16x32_bf16 v[184:187], a[152:155], a[28:31], v[184:187]
  buffer_load_dwordx4 v52, s[24:27], s70 offen offset:1024 lds
  v_mfma_f32_16x16x32_bf16 v[188:191], a[140:143], a[24:27], v[188:191]
  v_mfma_f32_16x16x32_bf16 v[188:191], a[156:159], a[28:31], v[188:191]
  buffer_load_dwordx4 v53, s[24:27], s70 offen offset:2048 lds
  v_mfma_f32_16x16x32_bf16 a[36:39], a[52:55], a[24:27], a[36:39]
  v_mfma_f32_16x16x32_bf16 a[36:39], a[52:55], a[28:31], a[36:39]
  s_waitcnt lgkmcnt(0)
  v_mfma_f32_16x16x32_bf16 v[192:195], a[128:131], a[0:3], v[192:195]
  buffer_load_dwordx4 v54, s[24:27], s70 offen offset:3072 lds
  v_mfma_f32_16x16x32_bf16 v[192:195], a[144:147], a[4:7], v[192:195]
  s_add_u32 m0, s63, 0x7000
  v_mfma_f32_16x16x32_bf16 v[196:199], a[132:135], a[0:3], v[196:199]
  v_mfma_f32_16x16x32_bf16 v[196:199], a[148:151], a[4:7], v[196:199]
  s_add_u32 s70, s68, s69
  v_mfma_f32_16x16x32_bf16 v[200:203], a[136:139], a[0:3], v[200:203]
LS8_%=:
  s_cbranch_execz LS12_%=
  buffer_load_dwordx4 v51, s[24:27], s70 offen lds
  v_mfma_f32_16x16x32_bf16 v[200:203], a[152:155], a[4:7], v[200:203]
  v_mfma_f32_16x16x32_bf16 v[204:207], a[140:143], a[0:3], v[204:207]
  buffer_load_dwordx4 v52, s[24:27], s70 offen offset:1024 lds
  v_mfma_f32_16x16x32_bf16 v[204:207], a[156:159], a[4:7], v[204:207]
  buffer_load_dwordx4 v53, s[24:27], s70 offen offset:2048 lds
  v_mfma_f32_16x16x32_bf16 a[40:43], a[48:51], a[0:3], a[40:43]
  s_waitcnt vmcnt(23)
  ds_read_b128 a[16:19], v49 offset:12288
  v_mfma_f32_16x16x32_bf16 a[40:43], a[48:51], a[4:7], a[40:43]
  buffer_load_dwordx4 v54, s[24:27], s70 offen offset:3072 lds
  v_mfma_f32_16x16x32_bf16 v[208:211], a[128:131], a[8:11], v[208:211]
  ds_read_b128 a[20:23], v50 offset:12288
  s_add_u32 s69, s61, 6
  v_mfma_f32_16x16x32_bf16 v[208:211], a[144:147], a[12:15], v[208:211]
  v_mfma_f32_16x16x32_bf16 v[212:215], a[132:135], a[8:11], v[212:215]
  ds_read_b128 a[24:27], v49 offset:14336
  s_and_b32 s69, s69, 7
  v_mfma_f32_16x16x32_bf16 v[212:215], a[148:151], a[12:15], v[212:215]
  s_lshl_b32 s69, s69, 7
  v_mfma_f32_16x16x32_bf16 v[216:219], a[136:139], a[8:11], v[216:219]
  ds_read_b128 a[28:31], v50 offset:14336
  v_mfma_f32_16x16x32_bf16 v[216:219], a[152:155], a[12:15], v[216:219]
  s_add_u32 m0, s63, 0
  v_mfma_f32_16x16x32_bf16 v[220:223], a[140:143], a[8:11], v[220:223]
  s_add_u32 s70, s65, s69
  v_mfma_f32_16x16x32_bf16 v[220:223], a[156:159], a[12:15], v[220:223]
  v_mfma_f32_16x16x32_bf16 a[40:43], a[52:55], a[8:11], a[40:43]
  buffer_load_dwordx4 v51, s[20:23], s70 offen lds
  v_mfma_f32_16x16x32_bf16 a[40:43], a[52:55], a[12:15], a[40:43]
  s_waitcnt lgkmcnt(0)
  v_mfma_f32_16x16x32_bf16 v[224:227], a[128:131], a[16:19], v[224:227]
  buffer_load_dwordx4 v52, s[20:23], s70 offen offset:1024 lds
  v_mfma_f32_16x16x32_bf16 v[224:227], a[144:147], a[20:23], v[224:227]
  v_mfma_f32_16x16x32_bf16 v[228:231], a[132:135], a[16:19], v[228:231]
  buffer_load_dwordx4 v53, s[20:23], s70 offen offset:2048 lds
  v_mfma_f32_16x16x32_bf16 v[228:231], a[148:151], a[20:23], v[228:231]
  v_mfma_f32_16x16x32_bf16 v[232:235], a[136:139], a[16:19], v[232:235]
  buffer_load_dwordx4 v54, s[20:23], s70 offen offset:3072 lds
  v_mfma_f32_16x16x32_bf16 v[232:235], a[152:155], a[20:23], v[232:235]
  v_mfma_f32_16x16x32_bf16 v[236:239], a[140:143], a[16:19], v[236:239]
  s_add_u32 s15, s61, 6
  v_mfma_f32_16x16x32_bf16 v[236:239], a[156:159], a[20:23], v[236:239]
  v_mfma_f32_16x16x32_bf16 a[44:47], a[48:51], a[16:19], a[44:47]
  s_waitcnt vmcnt(24)
  ds_read_b128 a[0:3], v49 offset:16384
  s_and_b32 s15, s15, 7
  v_mfma_f32_16x16x32_bf16 a[44:47], a[48:51], a[20:23], a[44:47]
  v_mfma_f32_16x16x32_bf16 v[240:243], a[128:131], a[24:27], v[240:243]
  ds_read_b128 a[4:7], v50 offset:16384
  v_mfma_f32_16x16x32_bf16 v[240:243], a[144:147], a[28:31], v[240:243]
  s_lshl_b32 s15, s15, 13
  v_mfma_f32_16x16x32_bf16 v[244:247], a[132:135], a[24:27], v[244:247]
  ds_read_b128 a[8:11], v49 offset:18432
  v_mfma_f32_16x16x32_bf16 v[244:247], a[148:151], a[28:31], v[244:247]
  s_add_u32 s15, s15, s14
  v_mfma_f32_16x16x32_bf16 v[248:251], a[136:139], a[24:27], v[248:251]
  ds_read_b128 a[12:15], v50 offset:18432
  v_mfma_f32_16x16x32_bf16 v[248:251], a[152:155], a[28:31], v[248:251]
  buffer_load_dwordx4 a[96:99], v2, s[8:11], s15 offen sc1
  v_mfma_f32_16x16x32_bf16 v[252:255], a[140:143], a[24:27], v[252:255]
  v_mfma_f32_16x16x32_bf16 v[252:255], a[156:159], a[28:31], v[252:255]
  buffer_load_dwordx4 a[112:115], v10, s[8:11], s15 offen sc1
  v_mfma_f32_16x16x32_bf16 a[44:47], a[52:55], a[24:27], a[44:47]
  v_mfma_f32_16x16x32_bf16 a[44:47], a[52:55], a[28:31], a[44:47]
  s_waitcnt lgkmcnt(0)
  s_waitcnt vmcnt(25)
  v_mfma_f32_16x16x32_bf16 v[128:131], a[160:163], a[0:3], v[128:131]
  buffer_load_dwordx4 a[100:103], v2, s[8:11], s15 offen offset:1024 sc1
  s_waitcnt vmcnt(25)
  v_mfma_f32_16x16x32_bf16 v[128:131], a[176:179], a[4:7], v[128:131]
  s_waitcnt vmcnt(24)
  v_mfma_f32_16x16x32_bf16 v[132:135], a[164:167], a[0:3], v[132:135]
  s_waitcnt vmcnt(23)
  v_mfma_f32_16x16x32_bf16 v[132:135], a[180:183], a[4:7], v[132:135]
  buffer_load_dwordx4 a[116:119], v10, s[8:11], s15 offen offset:1024 sc1
  s_waitcnt vmcnt(23)
  v_mfma_f32_16x16x32_bf16 v[136:139], a[168:171], a[0:3], v[136:139]
  s_waitcnt vmcnt(22)
  v_mfma_f32_16x16x32_bf16 v[136:139], a[184:187], a[4:7], v[136:139]
  s_waitcnt vmcnt(21)
  v_mfma_f32_16x16x32_bf16 v[140:143], a[172:175], a[0:3], v[140:143]
  buffer_load_dwordx4 a[104:107], v2, s[8:11], s15 offen offset:2048 sc1
  s_waitcnt vmcnt(21)
  v_mfma_f32_16x16x32_bf16 v[140:143], a[188:191], a[4:7], v[140:143]
  v_mfma_f32_16x16x32_bf16 a[32:35], a[48:51], a[0:3], a[32:35]
  s_waitcnt vmcnt(17)
  ds_read_b128 a[16:19], v49 offset:20480
  v_mfma_f32_16x16x32_bf16 a[32:35], a[48:51], a[4:7], a[32:35]
  v_mfma_f32_16x16x32_bf16 v[144:147], a[160:163], a[8:11], v[144:147]
  ds_read_b128 a[20:23], v50 offset:20480
  v_mfma_f32_16x16x32_bf16 v[144:147], a[176:179], a[12:15], v[144:147]
  buffer_load_dwordx4 a[120:123], v10, s[8:11], s15 offen offset:2048 sc1
  v_mfma_f32_16x16x32_bf16 v[148:151], a[164:167], a[8:11], v[148:151]
  ds_read_b128 a[24:27], v49 offset:22528
  v_mfma_f32_16x16x32_bf16 v[148:151], a[180:183], a[12:15], v[148:151]
  buffer_load_dwordx4 a[108:111], v2, s[8:11], s15 offen offset:3072 sc1
  v_mfma_f32_16x16x32_bf16 v[152:155], a[168:171], a[8:11], v[152:155]
  ds_read_b128 a[28:31], v50 offset:22528
  v_mfma_f32_16x16x32_bf16 v[152:155], a[184:187], a[12:15], v[152:155]
  v_mfma_f32_16x16x32_bf16 v[156:159], a[172:175], a[8:11], v[156:159]
  buffer_load_dwordx4 a[124:127], v10, s[8:11], s15 offen offset:3072 sc1
  v_mfma_f32_16x16x32_bf16 v[156:159], a[188:191], a[12:15], v[156:159]
  v_mfma_f32_16x16x32_bf16 a[32:35], a[52:55], a[8:11], a[32:35]
  v_mfma_f32_16x16x32_bf16 a[32:35], a[52:55], a[12:15], a[32:35]
  s_waitcnt lgkmcnt(0)
  v_mfma_f32_16x16x32_bf16 v[160:163], a[160:163], a[16:19], v[160:163]
  s_add_u32 m0, s63, 0x1000
  v_mfma_f32_16x16x32_bf16 v[160:163], a[176:179], a[20:23], v[160:163]
  v_mfma_f32_16x16x32_bf16 v[164:167], a[164:167], a[16:19], v[164:167]
  s_add_u32 s70, s68, s69
  v_mfma_f32_16x16x32_bf16 v[164:167], a[180:183], a[20:23], v[164:167]
  buffer_load_dwordx2 v[20:21], v77, s[8:11], s48 offen sc1
  v_mfma_f32_16x16x32_bf16 v[168:171], a[168:171], a[16:19], v[168:171]
  buffer_load_dwordx4 v51, s[20:23], s70 offen lds
  v_mfma_f32_16x16x32_bf16 v[168:171], a[184:187], a[20:23], v[168:171]
  v_mfma_f32_16x16x32_bf16 v[172:175], a[172:175], a[16:19], v[172:175]
  buffer_load_dwordx4 v52, s[20:23], s70 offen offset:1024 lds
  v_mfma_f32_16x16x32_bf16 v[172:175], a[188:191], a[20:23], v[172:175]
  v_mfma_f32_16x16x32_bf16 a[36:39], a[48:51], a[16:19], a[36:39]
  s_waitcnt vmcnt(19)
  ds_read_b128 a[0:3], v49 offset:24576
  v_mfma_f32_16x16x32_bf16 a[36:39], a[48:51], a[20:23], a[36:39]
  buffer_load_dwordx4 v53, s[20:23], s70 offen offset:2048 lds
  v_mfma_f32_16x16x32_bf16 v[176:179], a[160:163], a[24:27], v[176:179]
  ds_read_b128 a[4:7], v50 offset:24576
  v_mfma_f32_16x16x32_bf16 v[176:179], a[176:179], a[28:31], v[176:179]
  buffer_load_dwordx4 v54, s[20:23], s70 offen offset:3072 lds
  v_mfma_f32_16x16x32_bf16 v[180:183], a[164:167], a[24:27], v[180:183]
  ds_read_b128 a[8:11], v49 offset:26624
  v_mfma_f32_16x16x32_bf16 v[180:183], a[180:183], a[28:31], v[180:183]
  s_add_u32 m0, s63, 0x2000
  v_mfma_f32_16x16x32_bf16 v[184:187], a[168:171], a[24:27], v[184:187]
  ds_read_b128 a[12:15], v50 offset:26624
  v_mfma_f32_16x16x32_bf16 v[184:187], a[184:187], a[28:31], v[184:187]
  s_add_u32 s70, s65, s69
  v_mfma_f32_16x16x32_bf16 v[188:191], a[172:175], a[24:27], v[188:191]
  v_mfma_f32_16x16x32_bf16 v[188:191], a[188:191], a[28:31], v[188:191]
  buffer_load_dwordx4 v51, s[24:27], s70 offen lds
  v_mfma_f32_16x16x32_bf16 a[36:39], a[52:55], a[24:27], a[36:39]
LS9_%=:
  s_cbranch_execz LS13_%=
  v_mfma_f32_16x16x32_bf16 a[36:39], a[52:55], a[28:31], a[36:39]
  s_waitcnt lgkmcnt(0)
  v_mfma_f32_16x16x32_bf16 v[192:195], a[160:163], a[0:3], v[192:195]
  buffer_load_dwordx4 v52, s[24:27], s70 offen offset:1024 lds
  v_mfma_f32_16x16x32_bf16 v[192:195], a[176:179], a[4:7], v[192:195]
  v_mfma_f32_16x16x32_bf16 v[196:199], a[164:167], a[0:3], v[196:199]
  buffer_load_dwordx4 v53, s[24:27], s70 offen offset:2048 lds
  v_mfma_f32_16x16x32_bf16 v[196:199], a[180:183], a[4:7], v[196:199]
  v_mfma_f32_16x16x32_bf16 v[200:203], a[168:171], a[0:3], v[200:203]
  v_mfma_f32_16x16x32_bf16 v[200:203], a[184:187], a[4:7], v[200:203]
  buffer_load_dwordx4 v54, s[24:27], s70 offen offset:3072 lds
  v_mfma_f32_16x16x32_bf16 v[204:207], a[172:175], a[0:3], v[204:207]
  v_mfma_f32_16x16x32_bf16 v[204:207], a[188:191], a[4:7], v[204:207]
  s_add_u32 m0, s63, 0x3000
  v_mfma_f32_16x16x32_bf16 a[40:43], a[48:51], a[0:3], a[40:43]
  s_waitcnt vmcnt(21)
  ds_read_b128 a[16:19], v49 offset:28672
  v_mfma_f32_16x16x32_bf16 a[40:43], a[48:51], a[4:7], a[40:43]
  v_mfma_f32_16x16x32_bf16 v[208:211], a[160:163], a[8:11], v[208:211]
  ds_read_b128 a[20:23], v50 offset:28672
  s_add_u32 s70, s68, s69
  v_mfma_f32_16x16x32_bf16 v[208:211], a[176:179], a[12:15], v[208:211]
  v_mfma_f32_16x16x32_bf16 v[212:215], a[164:167], a[8:11], v[212:215]
  ds_read_b128 a[24:27], v49 offset:30720
  v_mfma_f32_16x16x32_bf16 v[212:215], a[180:183], a[12:15], v[212:215]
  buffer_load_dwordx4 v51, s[24:27], s70 offen lds
  v_mfma_f32_16x16x32_bf16 v[216:219], a[168:171], a[8:11], v[216:219]
  ds_read_b128 a[28:31], v50 offset:30720
  v_mfma_f32_16x16x32_bf16 v[216:219], a[184:187], a[12:15], v[216:219]
  buffer_load_dwordx4 v52, s[24:27], s70 offen offset:1024 lds
  v_mfma_f32_16x16x32_bf16 v[220:223], a[172:175], a[8:11], v[220:223]
  v_mfma_f32_16x16x32_bf16 v[220:223], a[188:191], a[12:15], v[220:223]
  buffer_load_dwordx4 v53, s[24:27], s70 offen offset:2048 lds
  v_mfma_f32_16x16x32_bf16 a[40:43], a[52:55], a[8:11], a[40:43]
  v_mfma_f32_16x16x32_bf16 a[40:43], a[52:55], a[12:15], a[40:43]
  s_waitcnt lgkmcnt(0)
  v_mfma_f32_16x16x32_bf16 v[224:227], a[160:163], a[16:19], v[224:227]
  buffer_load_dwordx4 v54, s[24:27], s70 offen offset:3072 lds
  v_mfma_f32_16x16x32_bf16 v[224:227], a[176:179], a[20:23], v[224:227]
  v_mfma_f32_16x16x32_bf16 v[228:231], a[164:167], a[16:19], v[228:231]
  s_add_u32 s69, s61, 7
  v_mfma_f32_16x16x32_bf16 v[228:231], a[180:183], a[20:23], v[228:231]
  v_mfma_f32_16x16x32_bf16 v[232:235], a[168:171], a[16:19], v[232:235]
  s_and_b32 s69, s69, 7
  v_mfma_f32_16x16x32_bf16 v[232:235], a[184:187], a[20:23], v[232:235]
  v_mfma_f32_16x16x32_bf16 v[236:239], a[172:175], a[16:19], v[236:239]
  s_lshl_b32 s69, s69, 7
  v_mfma_f32_16x16x32_bf16 v[236:239], a[188:191], a[20:23], v[236:239]
  v_mfma_f32_16x16x32_bf16 a[44:47], a[48:51], a[16:19], a[44:47]
  s_waitcnt vmcnt(21)
  ds_read_b128 a[0:3], v49
  s_add_u32 m0, s63, 0x4000
  v_mfma_f32_16x16x32_bf16 a[44:47], a[48:51], a[20:23], a[44:47]
  v_mfma_f32_16x16x32_bf16 v[240:243], a[160:163], a[24:27], v[240:243]
  ds_read_b128 a[4:7], v50
  s_add_u32 s70, s65, s69
  v_mfma_f32_16x16x32_bf16 v[240:243], a[176:179], a[28:31], v[240:243]
  v_mfma_f32_16x16x32_bf16 v[244:247], a[164:167], a[24:27], v[244:247]
  ds_read_b128 a[8:11], v49 offset:2048
  v_mfma_f32_16x16x32_bf16 v[244:247], a[180:183], a[28:31], v[244:247]
  buffer_load_dwordx4 v51, s[20:23], s70 offen lds
  v_mfma_f32_16x16x32_bf16 v[248:251], a[168:171], a[24:27], v[248:251]
  ds_read_b128 a[12:15], v50 offset:2048
  v_mfma_f32_16x16x32_bf16 v[248:251], a[184:187], a[28:31], v[248:251]
  buffer_load_dwordx4 v52, s[20:23], s70 offen offset:1024 lds
  v_mfma_f32_16x16x32_bf16 v[252:255], a[172:175], a[24:27], v[252:255]
  buffer_load_dwordx4 v53, s[20:23], s70 offen offset:2048 lds
  v_mfma_f32_16x16x32_bf16 v[252:255], a[188:191], a[28:31], v[252:255]
  v_mfma_f32_16x16x32_bf16 a[44:47], a[52:55], a[24:27], a[44:47]
  buffer_load_dwordx4 v54, s[20:23], s70 offen offset:3072 lds
  v_mfma_f32_16x16x32_bf16 a[44:47], a[52:55], a[28:31], a[44:47]
  s_waitcnt lgkmcnt(0)
  s_waitcnt vmcnt(24)
  v_mfma_f32_16x16x32_bf16 v[128:131], a[96:99], a[0:3], v[128:131]
  s_add_u32 s15, s61, 7
  s_waitcnt vmcnt(23)
  v_mfma_f32_16x16x32_bf16 v[128:131], a[112:115], a[4:7], v[128:131]
  s_waitcnt vmcnt(22)
  v_mfma_f32_16x16x32_bf16 v[132:135], a[100:103], a[0:3], v[132:135]
  s_and_b32 s15, s15, 7
  s_waitcnt vmcnt(21)
  v_mfma_f32_16x16x32_bf16 v[132:135], a[116:119], a[4:7], v[132:135]
  s_waitcnt vmcnt(16)
  v_cmp_ne_u64 vcc, s[66:67], v[20:21]
  s_cmp_lg_u64 vcc, 0
  s_cbranch_scc1 L7_%=
L8_%=:
  v_mfma_f32_16x16x32_bf16 v[136:139], a[104:107], a[0:3], v[136:139]
  s_lshl_b32 s15, s15, 13
  v_mfma_f32_16x16x32_bf16 v[136:139], a[120:123], a[4:7], v[136:139]
  v_mfma_f32_16x16x32_bf16 v[140:143], a[108:111], a[0:3], v[140:143]
  s_add_u32 s15, s15, s14
  v_mfma_f32_16x16x32_bf16 v[140:143], a[124:127], a[4:7], v[140:143]
  s_mov_b32 s49, 0x40000
  buffer_load_dwordx2 v[56:57], v78, s[8:11], s49 offen sc1
  v_mfma_f32_16x16x32_bf16 a[32:35], a[48:51], a[0:3], a[32:35]
  s_waitcnt vmcnt(13)
  ds_read_b128 a[16:19], v49 offset:4096
  v_mfma_f32_16x16x32_bf16 a[32:35], a[48:51], a[4:7], a[32:35]
  buffer_load_dwordx4 a[128:131], v2, s[8:11], s15 offen sc1
  v_mfma_f32_16x16x32_bf16 v[144:147], a[96:99], a[8:11], v[144:147]
  ds_read_b128 a[20:23], v50 offset:4096
  v_mfma_f32_16x16x32_bf16 v[144:147], a[112:115], a[12:15], v[144:147]
  buffer_load_dwordx4 a[144:147], v10, s[8:11], s15 offen sc1
  v_mfma_f32_16x16x32_bf16 v[148:151], a[100:103], a[8:11], v[148:151]
  ds_read_b128 a[24:27], v49 offset:6144
  v_mfma_f32_16x16x32_bf16 v[148:151], a[116:119], a[12:15], v[148:151]
  buffer_load_dwordx4 a[132:135], v2, s[8:11], s15 offen offset:1024 sc1
  v_mfma_f32_16x16x32_bf16 v[152:155], a[104:107], a[8:11], v[152:155]
  ds_read_b128 a[28:31], v50 offset:6144
  v_mfma_f32_16x16x32_bf16 v[152:155], a[120:123], a[12:15], v[152:155]
  buffer_load_dwordx4 a[148:151], v10, s[8:11], s15 offen offset:1024 sc1
  v_mfma_f32_16x16x32_bf16 v[156:159], a[108:111], a[8:11], v[156:159]
  buffer_load_dwordx4 a[136:139], v2, s[8:11], s15 offen offset:2048 sc1
  v_mfma_f32_16x16x32_bf16 v[156:159], a[124:127], a[12:15], v[156:159]
  s_mov_b32 s49, 0x43000
  buffer_load_dwordx2 v[58:59], v78, s[8:11], s49 offen sc1
  v_mfma_f32_16x16x32_bf16 a[32:35], a[52:55], a[8:11], a[32:35]
  buffer_load_dwordx4 a[152:155], v10, s[8:11], s15 offen offset:2048 sc1
  v_mfma_f32_16x16x32_bf16 a[32:35], a[52:55], a[12:15], a[32:35]
  s_mov_b32 s49, 0x46000
  buffer_load_dwordx2 v[60:61], v78, s[8:11], s49 offen sc1
  s_mov_b32 s49, 0x49000
  buffer_load_dwordx2 v[62:63], v78, s[8:11], s49 offen sc1
  s_waitcnt lgkmcnt(0)
  v_mfma_f32_16x16x32_bf16 v[160:163], a[96:99], a[16:19], v[160:163]
  buffer_load_dwordx4 a[140:143], v2, s[8:11], s15 offen offset:3072 sc1
  v_mfma_f32_16x16x32_bf16 v[160:163], a[112:115], a[20:23], v[160:163]
  v_mfma_f32_16x16x32_bf16 v[164:167], a[100:103], a[16:19], v[164:167]
  buffer_load_dwordx4 a[156:159], v10, s[8:11], s15 offen offset:3072 sc1
  v_mfma_f32_16x16x32_bf16 v[164:167], a[116:119], a[20:23], v[164:167]
  v_mfma_f32_16x16x32_bf16 v[168:171], a[104:107], a[16:19], v[168:171]
  v_mfma_f32_16x16x32_bf16 v[168:171], a[120:123], a[20:23], v[168:171]
  s_add_u32 m0, s63, 0x5000
  v_mfma_f32_16x16x32_bf16 v[172:175], a[108:111], a[16:19], v[172:175]
  v_mfma_f32_16x16x32_bf16 v[172:175], a[124:127], a[20:23], v[172:175]
  s_add_u32 s70, s68, s69
  v_mfma_f32_16x16x32_bf16 a[36:39], a[48:51], a[16:19], a[36:39]
  s_waitcnt vmcnt(20)
  ds_read_b128 a[0:3], v49 offset:8192
  v_mfma_f32_16x16x32_bf16 a[36:39], a[48:51], a[20:23], a[36:39]
  v_mfma_f32_16x16x32_bf16 v[176:179], a[96:99], a[24:27], v[176:179]
LS10_%=:
  s_cbranch_execz LS14_%=
  ds_read_b128 a[4:7], v50 offset:8192
  v_mfma_f32_16x16x32_bf16 v[176:179], a[112:115], a[28:31], v[176:179]
  buffer_load_dwordx4 v51, s[20:23], s70 offen lds
  v_mfma_f32_16x16x32_bf16 v[180:183], a[100:103], a[24:27], v[180:183]
  ds_read_b128 a[8:11], v49 offset:10240
  v_mfma_f32_16x16x32_bf16 v[180:183], a[116:119], a[28:31], v[180:183]
  buffer_load_dwordx4 v52, s[20:23], s70 offen offset:1024 lds
  v_mfma_f32_16x16x32_bf16 v[184:187], a[104:107], a[24:27], v[184:187]
  ds_read_b128 a[12:15], v50 offset:10240
  v_mfma_f32_16x16x32_bf16 v[184:187], a[120:123], a[28:31], v[184:187]
  buffer_load_dwordx4 v53, s[20:23], s70 offen offset:2048 lds
  v_mfma_f32_16x16x32_bf16 v[188:191], a[108:111], a[24:27], v[188:191]
  v_mfma_f32_16x16x32_bf16 v[188:191], a[124:127], a[28:31], v[188:191]
  buffer_load_dwordx4 v54, s[20:23], s70 offen offset:3072 lds
  v_mfma_f32_16x16x32_bf16 a[36:39], a[52:55], a[24:27], a[36:39]
  v_mfma_f32_16x16x32_bf16 a[36:39], a[52:55], a[28:31], a[36:39]
  s_waitcnt lgkmcnt(0)
  v_mfma_f32_16x16x32_bf16 v[192:195], a[96:99], a[0:3], v[192:195]
  s_add_u32 m0, s63, 0x6000
  v_mfma_f32_16x16x32_bf16 v[192:195], a[112:115], a[4:7], v[192:195]
  v_mfma_f32_16x16x32_bf16 v[196:199], a[100:103], a[0:3], v[196:199]
  s_add_u32 s70, s65, s69
  v_mfma_f32_16x16x32_bf16 v[196:199], a[116:119], a[4:7], v[196:199]
  v_mfma_f32_16x16x32_bf16 v[200:203], a[104:107], a[0:3], v[200:203]
  buffer_load_dwordx4 v51, s[24:27], s70 offen lds
  v_mfma_f32_16x16x32_bf16 v[200:203], a[120:123], a[4:7], v[200:203]
  v_mfma_f32_16x16x32_bf16 v[204:207], a[108:111], a[0:3], v[204:207]
  buffer_load_dwordx4 v52, s[24:27], s70 offen offset:1024 lds
  v_mfma_f32_16x16x32_bf16 v[204:207], a[124:127], a[4:7], v[204:207]
  v_mfma_f32_16x16x32_bf16 a[40:43], a[48:51], a[0:3], a[40:43]
  s_waitcnt vmcnt(22)
  ds_read_b128 a[16:19], v49 offset:12288
  v_mfma_f32_16x16x32_bf16 a[40:43], a[48:51], a[4:7], a[40:43]
  buffer_load_dwordx4 v53, s[24:27], s70 offen offset:2048 lds
  v_mfma_f32_16x16x32_bf16 v[208:211], a[96:99], a[8:11], v[208:211]
  ds_read_b128 a[20:23], v50 offset:12288
  v_mfma_f32_16x16x32_bf16 v[208:211], a[112:115], a[12:15], v[208:211]
  buffer_load_dwordx4 v54, s[24:27], s70 offen offset:3072 lds
  v_mfma_f32_16x16x32_bf16 v[212:215], a[100:103], a[8:11], v[212:215]
  ds_read_b128 a[24:27], v49 offset:14336
  v_mfma_f32_16x16x32_bf16 v[212:215], a[116:119], a[12:15], v[212:215]
  s_add_u32 m0, s63, 0x7000
  v_mfma_f32_16x16x32_bf16 v[216:219], a[104:107], a[8:11], v[216:219]
  ds_read_b128 a[28:31], v50 offset:14336
  v_mfma_f32_16x16x32_bf16 v[216:219], a[120:123], a[12:15], v[216:219]
  s_add_u32 s70, s68, s69
  v_mfma_f32_16x16x32_bf16 v[220:223], a[108:111], a[8:11], v[220:223]
  v_mfma_f32_16x16x32_bf16 v[220:223], a[124:127], a[12:15], v[220:223]
  buffer_load_dwordx4 v51, s[24:27], s70 offen lds
  v_mfma_f32_16x16x32_bf16 a[40:43], a[52:55], a[8:11], a[40:43]
  v_mfma_f32_16x16x32_bf16 a[40:43], a[52:55], a[12:15], a[40:43]
  s_waitcnt lgkmcnt(0)
  v_mfma_f32_16x16x32_bf16 v[224:227], a[96:99], a[16:19], v[224:227]
  buffer_load_dwordx4 v52, s[24:27], s70 offen offset:1024 lds
  v_mfma_f32_16x16x32_bf16 v[224:227], a[112:115], a[20:23], v[224:227]
  v_mfma_f32_16x16x32_bf16 v[228:231], a[100:103], a[16:19], v[228:231]
  v_mfma_f32_16x16x32_bf16 v[228:231], a[116:119], a[20:23], v[228:231]
  s_waitcnt vmcnt(15)
  v_add_f32 v56, v56, v58
  v_mfma_f32_16x16x32_bf16 v[232:235], a[104:107], a[16:19], v[232:235]
  buffer_load_dwordx4 v53, s[24:27], s70 offen offset:2048 lds
  v_mfma_f32_16x16x32_bf16 v[232:235], a[120:123], a[20:23], v[232:235]
  v_add_f32 v57, v57, v59
  v_mfma_f32_16x16x32_bf16 v[236:239], a[108:111], a[16:19], v[236:239]
  s_waitcnt vmcnt(14)
  v_add_f32 v56, v56, v60
  v_mfma_f32_16x16x32_bf16 v[236:239], a[124:127], a[20:23], v[236:239]
  v_add_f32 v57, v57, v61
  v_mfma_f32_16x16x32_bf16 a[44:47], a[48:51], a[16:19], a[44:47]
  ds_read_b128 a[0:3], v49 offset:16384
  v_mfma_f32_16x16x32_bf16 a[44:47], a[48:51], a[20:23], a[44:47]
  buffer_load_dwordx4 v54, s[24:27], s70 offen offset:3072 lds
  v_mfma_f32_16x16x32_bf16 v[240:243], a[96:99], a[24:27], v[240:243]
  ds_read_b128 a[4:7], v50 offset:16384
  s_waitcnt vmcnt(14)
  v_add_f32 v56, v56, v62
  v_mfma_f32_16x16x32_bf16 v[240:243], a[112:115], a[28:31], v[240:243]
  v_add_f32 v57, v57, v63
  v_mfma_f32_16x16x32_bf16 v[244:247], a[100:103], a[24:27], v[244:247]
  ds_read_b128 a[8:11], v49 offset:18432
  v_cvt_pk_bf16_f32 v58, v56, v57
  v_mfma_f32_16x16x32_bf16 v[244:247], a[116:119], a[28:31], v[244:247]
  s_lshl2_add_u32 s54, s2, s58
  s_mul_i32 s55, s54, 0x700
  v_min_u32 v3, 13, v1
  v_lshlrev_b32 v3, 7, v3
  buffer_load_dword v8, v3, s[36:39], s55 offen
  buffer_load_dword v8, v3, s[40:43], s55 offen
  v_mfma_f32_16x16x32_bf16 v[248:251], a[104:107], a[24:27], v[248:251]
  ds_read_b128 a[12:15], v50 offset:18432
  v_lshlrev_b32 v56, 16, v58
  v_mfma_f32_16x16x32_bf16 v[248:251], a[120:123], a[28:31], v[248:251]
  v_and_b32 v57, 0xffff0000, v58
  v_mfma_f32_16x16x32_bf16 v[252:255], a[108:111], a[24:27], v[252:255]
  buffer_load_dwordx4 v[98:101], v15, s[4:7], s74 offen
  buffer_load_dwordx4 v[102:105], v15, s[4:7], s75 offen
  v_mfma_f32_16x16x32_bf16 v[252:255], a[124:127], a[28:31], v[252:255]
  v_add_f32 v56, v56, v79
  v_mfma_f32_16x16x32_bf16 a[44:47], a[52:55], a[24:27], a[44:47]
  v_add_f32 v57, v57, v79
  v_mfma_f32_16x16x32_bf16 a[44:47], a[52:55], a[28:31], a[44:47]
  s_waitcnt lgkmcnt(0)
  v_mfma_f32_16x16x32_bf16 v[128:131], a[128:131], a[0:3], v[128:131]
  v_mfma_f32_16x16x32_bf16 v[128:131], a[144:147], a[4:7], v[128:131]
  v_mfma_f32_16x16x32_bf16 v[132:135], a[132:135], a[0:3], v[132:135]
  v_mfma_f32_16x16x32_bf16 v[132:135], a[148:151], a[4:7], v[132:135]
  v_cvt_pk_bf16_f32 v58, v56, v57
  v_mfma_f32_16x16x32_bf16 v[136:139], a[136:139], a[0:3], v[136:139]
  v_mfma_f32_16x16x32_bf16 v[136:139], a[152:155], a[4:7], v[136:139]
  v_lshlrev_b32 v56, 16, v58
  s_waitcnt vmcnt(17)
  v_mfma_f32_16x16x32_bf16 v[140:143], a[140:143], a[0:3], v[140:143]
  s_waitcnt vmcnt(16)
  v_mfma_f32_16x16x32_bf16 v[140:143], a[156:159], a[4:7], v[140:143]
  v_and_b32 v57, 0xffff0000, v58
  v_mfma_f32_16x16x32_bf16 a[32:35], a[48:51], a[0:3], a[32:35]
  s_waitcnt vmcnt(12)
  ds_read_b128 a[16:19], v49 offset:20480
  v_mfma_f32_16x16x32_bf16 a[32:35], a[48:51], a[4:7], a[32:35]
  v_xor_b32 v60, 0x80000000, v56
  v_mfma_f32_16x16x32_bf16 v[144:147], a[128:131], a[8:11], v[144:147]
  ds_read_b128 a[20:23], v50 offset:20480
  v_mfma_f32_16x16x32_bf16 v[144:147], a[144:147], a[12:15], v[144:147]
  v_xor_b32 v61, 0x80000000, v57
  v_mfma_f32_16x16x32_bf16 v[148:151], a[132:135], a[8:11], v[148:151]
  ds_read_b128 a[24:27], v49 offset:22528
  v_mfma_f32_16x16x32_bf16 v[148:151], a[148:151], a[12:15], v[148:151]
  ds_write_b64 v80, v[56:57]
  v_mfma_f32_16x16x32_bf16 v[152:155], a[136:139], a[8:11], v[152:155]
  ds_read_b128 a[28:31], v50 offset:22528
  v_mfma_f32_16x16x32_bf16 v[152:155], a[152:155], a[12:15], v[152:155]
  ds_write_b64 v80, v[60:61] offset:2048
  v_mfma_f32_16x16x32_bf16 v[156:159], a[140:143], a[8:11], v[156:159]
  v_mfma_f32_16x16x32_bf16 v[156:159], a[156:159], a[12:15], v[156:159]
  s_lshl4_add_u32 s52, s60, s59
  s_lshl_b32 s52, s52, 7
  s_add_u32 s52, s52, 0x60000
  buffer_store_dword v58, v81, s[8:11], s52 offen sc1
  v_mfma_f32_16x16x32_bf16 a[32:35], a[52:55], a[8:11], a[32:35]
  v_mfma_f32_16x16x32_bf16 a[32:35], a[52:55], a[12:15], a[32:35]
  s_waitcnt lgkmcnt(1)
LS11_%=:
  s_cbranch_execz LS15_%=
  v_mfma_f32_16x16x32_bf16 v[160:163], a[128:131], a[16:19], v[160:163]
  v_mfma_f32_16x16x32_bf16 v[160:163], a[144:147], a[20:23], v[160:163]
  v_mfma_f32_16x16x32_bf16 v[164:167], a[132:135], a[16:19], v[164:167]
  v_mfma_f32_16x16x32_bf16 v[164:167], a[148:151], a[20:23], v[164:167]
  v_cvt_pk_bf16_f32 v128, v128, v129
  v_cvt_pk_bf16_f32 v129, v130, v131
  v_mfma_f32_16x16x32_bf16 v[168:171], a[136:139], a[16:19], v[168:171]
  v_cvt_pk_bf16_f32 v130, v132, v133
  v_mfma_f32_16x16x32_bf16 v[168:171], a[152:155], a[20:23], v[168:171]
  v_cvt_pk_bf16_f32 v131, v134, v135
  v_mfma_f32_16x16x32_bf16 v[172:175], a[140:143], a[16:19], v[172:175]
  ds_write_b128 v11, v[128:131]
  v_mfma_f32_16x16x32_bf16 v[172:175], a[156:159], a[20:23], v[172:175]
  v_cvt_pk_bf16_f32 v132, v136, v137
  v_cvt_pk_bf16_f32 v133, v138, v139
  v_mfma_f32_16x16x32_bf16 a[36:39], a[48:51], a[16:19], a[36:39]
  s_waitcnt vmcnt(9)
  ds_read_b128 a[0:3], v49 offset:24576
  v_cvt_pk_bf16_f32 v134, v140, v141
  v_mfma_f32_16x16x32_bf16 a[36:39], a[48:51], a[20:23], a[36:39]
  v_cvt_pk_bf16_f32 v135, v142, v143
  v_mfma_f32_16x16x32_bf16 v[176:179], a[128:131], a[24:27], v[176:179]
  ds_read_b128 a[4:7], v50 offset:24576
  v_mfma_f32_16x16x32_bf16 v[176:179], a[144:147], a[28:31], v[176:179]
  ds_write_b128 v11, v[132:135] offset:1024
  v_mfma_f32_16x16x32_bf16 v[180:183], a[132:135], a[24:27], v[180:183]
  ds_read_b128 a[8:11], v49 offset:26624
  v_cvt_pk_bf16_f32 v136, v144, v145
  v_cvt_pk_bf16_f32 v137, v146, v147
  v_mfma_f32_16x16x32_bf16 v[180:183], a[148:151], a[28:31], v[180:183]
  v_cvt_pk_bf16_f32 v138, v148, v149
  v_cvt_pk_bf16_f32 v139, v150, v151
  v_mfma_f32_16x16x32_bf16 v[184:187], a[136:139], a[24:27], v[184:187]
  ds_read_b128 a[12:15], v50 offset:26624
  v_mfma_f32_16x16x32_bf16 v[184:187], a[152:155], a[28:31], v[184:187]
  ds_write_b128 v11, v[136:139] offset:2048
  v_mfma_f32_16x16x32_bf16 v[188:191], a[140:143], a[24:27], v[188:191]
  v_cvt_pk_bf16_f32 v140, v152, v153
  v_cvt_pk_bf16_f32 v141, v154, v155
  v_mfma_f32_16x16x32_bf16 v[188:191], a[156:159], a[28:31], v[188:191]
  v_cvt_pk_bf16_f32 v142, v156, v157
  v_cvt_pk_bf16_f32 v143, v158, v159
  v_mfma_f32_16x16x32_bf16 a[36:39], a[52:55], a[24:27], a[36:39]
  ds_write_b128 v11, v[140:143] offset:3072
  v_mfma_f32_16x16x32_bf16 a[36:39], a[52:55], a[28:31], a[36:39]
  ds_write_b64 v55, a[32:33]
  s_waitcnt lgkmcnt(3)
  v_mfma_f32_16x16x32_bf16 v[192:195], a[128:131], a[0:3], v[192:195]
  v_mfma_f32_16x16x32_bf16 v[192:195], a[144:147], a[4:7], v[192:195]
  v_mfma_f32_16x16x32_bf16 v[196:199], a[132:135], a[0:3], v[196:199]
  v_mfma_f32_16x16x32_bf16 v[196:199], a[148:151], a[4:7], v[196:199]
  v_cvt_pk_bf16_f32 v160, v160, v161
  v_cvt_pk_bf16_f32 v161, v162, v163
  v_mfma_f32_16x16x32_bf16 v[200:203], a[136:139], a[0:3], v[200:203]
  v_cvt_pk_bf16_f32 v162, v164, v165
  v_mfma_f32_16x16x32_bf16 v[200:203], a[152:155], a[4:7], v[200:203]
  v_cvt_pk_bf16_f32 v163, v166, v167
  v_mfma_f32_16x16x32_bf16 v[204:207], a[140:143], a[0:3], v[204:207]
  ds_write_b128 v11, v[160:163] offset:4096
  v_mfma_f32_16x16x32_bf16 v[204:207], a[156:159], a[4:7], v[204:207]
  v_cvt_pk_bf16_f32 v164, v168, v169
  v_cvt_pk_bf16_f32 v165, v170, v171
  v_mfma_f32_16x16x32_bf16 a[40:43], a[48:51], a[0:3], a[40:43]
  s_waitcnt vmcnt(5)
  ds_read_b128 a[16:19], v49 offset:28672
  v_cvt_pk_bf16_f32 v166, v172, v173
  v_mfma_f32_16x16x32_bf16 a[40:43], a[48:51], a[4:7], a[40:43]
  v_cvt_pk_bf16_f32 v167, v174, v175
  v_mfma_f32_16x16x32_bf16 v[208:211], a[128:131], a[8:11], v[208:211]
  ds_read_b128 a[20:23], v50 offset:28672
  v_mfma_f32_16x16x32_bf16 v[208:211], a[144:147], a[12:15], v[208:211]
  ds_write_b128 v11, v[164:167] offset:5120
  v_mfma_f32_16x16x32_bf16 v[212:215], a[132:135], a[8:11], v[212:215]
  ds_read_b128 a[24:27], v49 offset:30720
  v_cvt_pk_bf16_f32 v168, v176, v177
  v_cvt_pk_bf16_f32 v169, v178, v179
  v_mfma_f32_16x16x32_bf16 v[212:215], a[148:151], a[12:15], v[212:215]
  v_cvt_pk_bf16_f32 v170, v180, v181
  v_cvt_pk_bf16_f32 v171, v182, v183
  v_mfma_f32_16x16x32_bf16 v[216:219], a[136:139], a[8:11], v[216:219]
  ds_read_b128 a[28:31], v50 offset:30720
  v_mfma_f32_16x16x32_bf16 v[216:219], a[152:155], a[12:15], v[216:219]
  ds_write_b128 v11, v[168:171] offset:6144
  v_mfma_f32_16x16x32_bf16 v[220:223], a[140:143], a[8:11], v[220:223]
  v_cvt_pk_bf16_f32 v172, v184, v185
  v_cvt_pk_bf16_f32 v173, v186, v187
  v_mfma_f32_16x16x32_bf16 v[220:223], a[156:159], a[12:15], v[220:223]
  v_cvt_pk_bf16_f32 v174, v188, v189
  v_cvt_pk_bf16_f32 v175, v190, v191
  v_mfma_f32_16x16x32_bf16 a[40:43], a[52:55], a[8:11], a[40:43]
  ds_write_b128 v11, v[172:175] offset:7168
  v_mfma_f32_16x16x32_bf16 a[40:43], a[52:55], a[12:15], a[40:43]
  ds_write_b64 v55, a[36:37] offset:2048
  s_waitcnt lgkmcnt(3)
  v_mfma_f32_16x16x32_bf16 v[224:227], a[128:131], a[16:19], v[224:227]
  v_mfma_f32_16x16x32_bf16 v[224:227], a[144:147], a[20:23], v[224:227]
  v_mfma_f32_16x16x32_bf16 v[228:231], a[132:135], a[16:19], v[228:231]
  v_mfma_f32_16x16x32_bf16 v[228:231], a[148:151], a[20:23], v[228:231]
  v_cvt_pk_bf16_f32 v192, v192, v193
  v_cvt_pk_bf16_f32 v193, v194, v195
  v_mfma_f32_16x16x32_bf16 v[232:235], a[136:139], a[16:19], v[232:235]
  v_cvt_pk_bf16_f32 v194, v196, v197
  v_cvt_pk_bf16_f32 v195, v198, v199
  v_mfma_f32_16x16x32_bf16 v[232:235], a[152:155], a[20:23], v[232:235]
  ds_write_b128 v11, v[192:195] offset:8192
  v_mfma_f32_16x16x32_bf16 v[236:239], a[140:143], a[16:19], v[236:239]
  v_cvt_pk_bf16_f32 v196, v200, v201
  v_cvt_pk_bf16_f32 v197, v202, v203
  v_mfma_f32_16x16x32_bf16 v[236:239], a[156:159], a[20:23], v[236:239]
  v_cvt_pk_bf16_f32 v198, v204, v205
  v_cvt_pk_bf16_f32 v199, v206, v207
  v_mfma_f32_16x16x32_bf16 a[44:47], a[48:51], a[16:19], a[44:47]
  ds_write_b128 v11, v[196:199] offset:9216
  v_mfma_f32_16x16x32_bf16 a[44:47], a[48:51], a[20:23], a[44:47]
  v_cvt_pk_bf16_f32 v200, v208, v209
  v_cvt_pk_bf16_f32 v201, v210, v211
  v_mfma_f32_16x16x32_bf16 v[240:243], a[128:131], a[24:27], v[240:243]
  v_cvt_pk_bf16_f32 v202, v212, v213
  v_mfma_f32_16x16x32_bf16 v[240:243], a[144:147], a[28:31], v[240:243]
  v_cvt_pk_bf16_f32 v203, v214, v215
  v_mfma_f32_16x16x32_bf16 v[244:247], a[132:135], a[24:27], v[244:247]
  ds_write_b128 v11, v[200:203] offset:10240
  v_mfma_f32_16x16x32_bf16 v[244:247], a[148:151], a[28:31], v[244:247]
  v_cvt_pk_bf16_f32 v204, v216, v217
  v_cvt_pk_bf16_f32 v205, v218, v219
  v_mfma_f32_16x16x32_bf16 v[248:251], a[136:139], a[24:27], v[248:251]
  v_cvt_pk_bf16_f32 v206, v220, v221
  v_cvt_pk_bf16_f32 v207, v222, v223
  v_mfma_f32_16x16x32_bf16 v[248:251], a[152:155], a[28:31], v[248:251]
  ds_write_b128 v11, v[204:207] offset:11264
LS12_%=:
  s_cbranch_execz L3_%=
  ds_write_b64 v55, a[40:41] offset:4096
  v_mfma_f32_16x16x32_bf16 v[252:255], a[140:143], a[24:27], v[252:255]
  v_mfma_f32_16x16x32_bf16 v[252:255], a[156:159], a[28:31], v[252:255]
  v_mfma_f32_16x16x32_bf16 a[44:47], a[52:55], a[24:27], a[44:47]
  v_mfma_f32_16x16x32_bf16 a[44:47], a[52:55], a[28:31], a[44:47]
  s_waitcnt lgkmcnt(0)
  s_barrier
  ds_read_b128 a[64:67], v46
  ds_read_b128 a[68:71], v47 offset:4096
  ds_read_b128 a[72:75], v48 offset:8192
  ds_read_b64 v[208:209], v74
  ds_read_b64 v[210:211], v75
  ds_read_b64 v[212:213], v76
  ds_read_b128 v[144:147], v71 offset:256
  ds_read_b128 v[176:179], v71
  ds_read_b128 v[148:151], v71 offset:272
  ds_read_b128 v[180:183], v71 offset:16
  ds_read_b128 a[76:79], v46 offset:1024
  ds_read_b128 a[80:83], v47 offset:5120
  ds_read_b128 a[84:87], v48 offset:9216
  v_accvgpr_read_b32 v214, a44
  v_accvgpr_read_b32 v215, a45
  s_waitcnt lgkmcnt(12)
  v_mfma_f32_16x16x32_bf16 v[224:227], a[58:61], a[64:67], v[224:227]
  ds_read_b128 a[88:91], v46 offset:2048
  s_add_u32 m0, s63, 0x8000
  s_xor_b32 s17, s16, 0
  s_waitcnt lgkmcnt(12)
  v_mfma_f32_16x16x32_bf16 v[224:227], a[58:61], a[68:71], v[224:227]
  ds_read_b128 a[92:95], v47 offset:6144
  s_waitcnt lgkmcnt(12)
  v_mfma_f32_16x16x32_bf16 v[224:227], a[58:61], a[72:75], v[224:227]
  ds_read_b128 a[96:99], v48 offset:10240
  s_waitcnt lgkmcnt(11)
  v_add_f32 v208, v208, v210
  v_mfma_f32_16x16x32_bf16 v[228:231], a[56:59], a[64:67], v[228:231]
  ds_read_b128 v[152:155], v71 offset:384
  v_add_f32 v209, v209, v211
  v_mfma_f32_16x16x32_bf16 v[228:231], a[56:59], a[68:71], v[228:231]
  ds_read_b128 v[184:187], v71 offset:128
  s_waitcnt lgkmcnt(12)
  v_add_f32 v208, v208, v212
  v_mfma_f32_16x16x32_bf16 v[228:231], a[56:59], a[72:75], v[228:231]
  ds_read_b128 v[156:159], v71 offset:400
  v_add_f32 v209, v209, v213
  s_waitcnt lgkmcnt(8)
  v_mfma_f32_16x16x32_bf16 v[232:235], a[58:61], a[76:79], v[232:235]
  ds_read_b128 v[188:191], v71 offset:144
  v_add_f32 v208, v208, v214
  s_waitcnt lgkmcnt(8)
  v_mfma_f32_16x16x32_bf16 v[232:235], a[58:61], a[80:83], v[232:235]
  ds_read_b128 a[100:103], v46 offset:3072
  v_add_f32 v209, v209, v215
  s_waitcnt lgkmcnt(8)
  v_mfma_f32_16x16x32_bf16 v[232:235], a[58:61], a[84:87], v[232:235]
  ds_read_b128 a[104:107], v47 offset:7168
  v_mul_f32 v224, v224, v144
  v_mul_f32 v225, v225, v145
  v_mfma_f32_16x16x32_bf16 v[236:239], a[56:59], a[76:79], v[236:239]
  ds_read_b128 a[108:111], v48 offset:11264
  v_mul_f32 v226, v226, v146
  v_mul_f32 v227, v227, v147
  v_mfma_f32_16x16x32_bf16 v[236:239], a[56:59], a[80:83], v[236:239]
  buffer_load_dwordx4 v67, s[28:31], s17 offen lds
  v_mfma_f32_16x16x32_bf16 v[236:239], a[56:59], a[84:87], v[236:239]
  buffer_load_dwordx4 v68, s[28:31], s17 offen offset:1024 lds
  s_waitcnt lgkmcnt(9)
  v_mfma_f32_16x16x32_bf16 v[240:243], a[58:61], a[88:91], v[240:243]
  v_mul_f32 v228, v228, v148
  v_mul_f32 v229, v229, v149
  s_waitcnt lgkmcnt(8)
  v_mfma_f32_16x16x32_bf16 v[240:243], a[58:61], a[92:95], v[240:243]
  v_mul_f32 v230, v230, v150
  v_mul_f32 v231, v231, v151
  s_waitcnt lgkmcnt(7)
  v_mfma_f32_16x16x32_bf16 v[240:243], a[58:61], a[96:99], v[240:243]
  v_fmac_f32 v224, v176, v208
  v_fmac_f32 v225, v177, v208
  v_mfma_f32_16x16x32_bf16 v[244:247], a[56:59], a[88:91], v[244:247]
  buffer_load_dwordx4 v69, s[28:31], s17 offen offset:2048 lds
  v_fmac_f32 v226, v178, v208
  v_fmac_f32 v227, v179, v208
  v_mfma_f32_16x16x32_bf16 v[244:247], a[56:59], a[92:95], v[244:247]
  v_fmac_f32 v228, v180, v208
  v_fmac_f32 v229, v181, v208
  v_fmac_f32 v230, v182, v208
  v_mfma_f32_16x16x32_bf16 v[244:247], a[56:59], a[96:99], v[244:247]
  v_fmac_f32 v231, v183, v208
  v_cvt_pk_bf16_f32 v128, v224, v225
  v_cvt_pk_bf16_f32 v129, v226, v227
  s_waitcnt lgkmcnt(2)
  v_mfma_f32_16x16x32_bf16 v[248:251], a[58:61], a[100:103], v[248:251]
  buffer_load_dwordx4 v70, s[28:31], s17 offen offset:3072 lds
  v_cvt_pk_bf16_f32 v130, v228, v229
  v_cvt_pk_bf16_f32 v131, v230, v231
  buffer_store_dwordx4 v[128:131], v2, s[8:11], s18 offen sc1
  s_waitcnt lgkmcnt(1)
  v_mfma_f32_16x16x32_bf16 v[248:251], a[58:61], a[104:107], v[248:251]
  v_mul_f32 v232, v232, v152
  v_mul_f32 v233, v233, v153
  s_waitcnt lgkmcnt(0)
  v_mfma_f32_16x16x32_bf16 v[248:251], a[58:61], a[108:111], v[248:251]
  v_mul_f32 v234, v234, v154
  s_add_u32 m0, s63, 0x3000
  v_mfma_f32_16x16x32_bf16 v[252:255], a[56:59], a[100:103], v[252:255]
  s_xor_b32 s17, s16, 0x8000
  v_mul_f32 v235, v235, v155
  v_mul_f32 v236, v236, v156
  v_mfma_f32_16x16x32_bf16 v[252:255], a[56:59], a[104:107], v[252:255]
  buffer_load_dwordx4 v67, s[28:31], s17 offen lds
  v_mul_f32 v237, v237, v157
  v_mul_f32 v238, v238, v158
  v_mfma_f32_16x16x32_bf16 v[252:255], a[56:59], a[108:111], v[252:255]
  v_mul_f32 v239, v239, v159
  v_fmac_f32 v232, v184, v208
  v_fmac_f32 v233, v185, v208
  v_fmac_f32 v234, v186, v208
  v_fmac_f32 v235, v187, v208
  v_fmac_f32 v236, v188, v208
  v_fmac_f32 v237, v189, v208
  v_fmac_f32 v238, v190, v208
  v_fmac_f32 v239, v191, v208
  v_cvt_pk_bf16_f32 v132, v232, v233
  buffer_load_dwordx4 v68, s[28:31], s17 offen offset:1024 lds
  v_cvt_pk_bf16_f32 v133, v234, v235
  v_cvt_pk_bf16_f32 v134, v236, v237
  v_cvt_pk_bf16_f32 v135, v238, v239
  buffer_store_dwordx4 v[132:135], v2, s[8:11], s18 offen offset:1024 sc1
  v_pk_mul_f32 v[240:241], v[240:241], v[144:145]
  v_pk_mul_f32 v[242:243], v[242:243], v[146:147]
  v_pk_mul_f32 v[244:245], v[244:245], v[148:149]
  v_pk_mul_f32 v[246:247], v[246:247], v[150:151]
  v_pk_fma_f32 v[240:241], v[176:177], v[208:209], v[240:241] op_sel:[0,1,0] op_sel_hi:[1,1,1]
  v_pk_fma_f32 v[242:243], v[178:179], v[208:209], v[242:243] op_sel:[0,1,0] op_sel_hi:[1,1,1]
  v_pk_fma_f32 v[244:245], v[180:181], v[208:209], v[244:245] op_sel:[0,1,0] op_sel_hi:[1,1,1]
  v_pk_fma_f32 v[246:247], v[182:183], v[208:209], v[246:247] op_sel:[0,1,0] op_sel_hi:[1,1,1]
  buffer_load_dwordx4 v69, s[28:31], s17 offen offset:2048 lds
  v_cvt_pk_bf16_f32 v136, v240, v241
  v_cvt_pk_bf16_f32 v137, v242, v243
  v_cvt_pk_bf16_f32 v138, v244, v245
  v_cvt_pk_bf16_f32 v139, v246, v247
  buffer_store_dwordx4 v[136:139], v2, s[8:11], s19 offen sc1
  v_pk_mul_f32 v[248:249], v[248:249], v[152:153]
  v_pk_mul_f32 v[250:251], v[250:251], v[154:155]
  v_pk_mul_f32 v[252:253], v[252:253], v[156:157]
  v_pk_mul_f32 v[254:255], v[254:255], v[158:159]
  v_pk_fma_f32 v[248:249], v[184:185], v[208:209], v[248:249] op_sel:[0,1,0] op_sel_hi:[1,1,1]
  v_pk_fma_f32 v[250:251], v[186:187], v[208:209], v[250:251] op_sel:[0,1,0] op_sel_hi:[1,1,1]
  buffer_load_dwordx4 v70, s[28:31], s17 offen offset:3072 lds
  v_pk_fma_f32 v[252:253], v[188:189], v[208:209], v[252:253] op_sel:[0,1,0] op_sel_hi:[1,1,1]
  v_pk_fma_f32 v[254:255], v[190:191], v[208:209], v[254:255] op_sel:[0,1,0] op_sel_hi:[1,1,1]
  v_cvt_pk_bf16_f32 v140, v248, v249
  v_cvt_pk_bf16_f32 v141, v250, v251
  v_cvt_pk_bf16_f32 v142, v252, v253
  v_cvt_pk_bf16_f32 v143, v254, v255
LS13_%=:
  s_cbranch_execz L3_%=
  buffer_store_dwordx4 v[140:143], v2, s[8:11], s19 offen offset:1024 sc1
  s_barrier
  s_add_u32 m0, s63, 0x7000
  s_xor_b32 s17, s16, 0
  buffer_load_dwordx4 v67, s[32:35], s17 offen lds
  buffer_load_dwordx4 v68, s[32:35], s17 offen offset:1024 lds
  buffer_load_dwordx4 v69, s[32:35], s17 offen offset:2048 lds
  buffer_load_dwordx4 v70, s[32:35], s17 offen offset:3072 lds
  s_add_u32 m0, s63, 0
  s_xor_b32 s17, s16, 0x8000
  buffer_load_dwordx4 v67, s[32:35], s17 offen lds
  buffer_load_dwordx4 v68, s[32:35], s17 offen offset:1024 lds
  buffer_load_dwordx4 v69, s[32:35], s17 offen offset:2048 lds
  buffer_load_dwordx4 v70, s[32:35], s17 offen offset:3072 lds
  s_waitcnt vmcnt(8)
  s_barrier
  buffer_store_dwordx2 v[18:19], v9, s[8:11], s50 offen sc1
  s_add_u32 s50, s50, 0x8000
  s_add_u32 s50, s71, 0x8000
  ds_read_b128 v[82:85], v72 offset:512
  ds_read_b128 v[86:89], v72 offset:576
  ds_read_b128 v[90:93], v72 offset:1024
  ds_read_b128 v[94:97], v72 offset:1088
  ds_read_b128 v[22:25], v72 offset:1280
  ds_read_b128 v[26:29], v72 offset:1344
  v_add_u32 v46, 0x4000, v46
  v_add_u32 v47, 0x4000, v47
  v_add_u32 v48, 0x4000, v48
  v_add_u32 v11, 0x4000, v11
  s_cmp_eq_u32 s2, 0
  s_cselect_b32 s52, 0, 0x40000000
  v_add_u32 v3, s52, v65
  buffer_store_dwordx2 v[18:19], v3, s[8:11], 0 offen sc1
  s_add_u32 m0, s63, 0x1000
  s_xor_b32 s17, s16, 0x80
  buffer_load_dwordx4 v67, s[28:31], s17 offen lds
  buffer_load_dwordx4 v68, s[28:31], s17 offen offset:1024 lds
  buffer_load_dwordx4 v69, s[28:31], s17 offen offset:2048 lds
  buffer_load_dwordx4 v70, s[28:31], s17 offen offset:3072 lds
  ds_read_b128 a[192:195], v49 offset:32768
  ds_read_b128 a[196:199], v50 offset:32768
  ds_read_b128 a[200:203], v49 offset:34816
  ds_read_b128 a[204:207], v50 offset:34816
  ds_read_b128 a[208:211], v49 offset:12288
  ds_read_b128 a[212:215], v50 offset:12288
  ds_read_b128 a[216:219], v49 offset:14336
  ds_read_b128 a[220:223], v50 offset:14336
  s_waitcnt vmcnt(10)
  ds_read_b128 a[224:227], v49 offset:28672
  ds_read_b128 a[228:231], v50 offset:28672
  ds_read_b128 a[232:235], v49 offset:30720
  ds_read_b128 a[236:239], v50 offset:30720
  s_waitcnt vmcnt(6)
  ds_read_b128 a[240:243], v49
  ds_read_b128 a[244:247], v50
  ds_read_b128 a[248:251], v49 offset:2048
  ds_read_b128 a[252:255], v50 offset:2048
  s_add_u32 m0, s63, 0x2000
  s_xor_b32 s17, s16, 0x8080
  buffer_load_dwordx4 v67, s[28:31], s17 offen lds
  buffer_load_dwordx4 v68, s[28:31], s17 offen offset:1024 lds
  buffer_load_dwordx4 v69, s[28:31], s17 offen offset:2048 lds
  buffer_load_dwordx4 v70, s[28:31], s17 offen offset:3072 lds
  s_add_u32 m0, s63, 0x8000
  s_xor_b32 s17, s16, 0x80
  s_waitcnt lgkmcnt(12)
  buffer_load_dwordx4 v67, s[32:35], s17 offen lds
  buffer_load_dwordx4 v68, s[32:35], s17 offen offset:1024 lds
  buffer_load_dwordx4 v69, s[32:35], s17 offen offset:2048 lds
  buffer_load_dwordx4 v70, s[32:35], s17 offen offset:3072 lds
  s_waitcnt vmcnt(8)
  ds_read_b128 a[128:131], v49 offset:4096
  ds_read_b128 a[132:135], v50 offset:4096
  ds_read_b128 a[136:139], v49 offset:6144
  ds_read_b128 a[140:143], v50 offset:6144
  s_add_u32 m0, s63, 0x3000
  s_xor_b32 s17, s16, 0x8080
  s_waitcnt lgkmcnt(12)
  buffer_load_dwordx4 v67, s[32:35], s17 offen lds
  buffer_load_dwordx4 v68, s[32:35], s17 offen offset:1024 lds
  buffer_load_dwordx4 v69, s[32:35], s17 offen offset:2048 lds
  buffer_load_dwordx4 v70, s[32:35], s17 offen offset:3072 lds
  buffer_load_dwordx2 v[20:21], v12, s[8:11], s51 offen sc1
  s_add_u32 m0, s63, 0x7000
  s_add_u32 s72, s72, 0x1c000
  s_waitcnt lgkmcnt(8)
  buffer_load_dwordx4 v30, s[36:39], s72 offen lds
  buffer_load_dwordx4 v31, s[36:39], s72 offen offset:1024 lds
  buffer_load_dwordx4 v32, s[36:39], s72 offen offset:2048 lds
  buffer_load_dwordx4 v33, s[36:39], s72 offen offset:3072 lds
  s_waitcnt vmcnt(13)
  ds_read_b128 a[144:147], v49 offset:8192
  ds_read_b128 a[148:151], v50 offset:8192
  ds_read_b128 a[152:155], v49 offset:10240
  ds_read_b128 a[156:159], v50 offset:10240
  s_add_u32 m0, s63, 0
  s_add_u32 s72, s72, 0x1c000
  s_waitcnt lgkmcnt(8)
  buffer_load_dwordx4 v30, s[36:39], s72 offen lds
  buffer_load_dwordx4 v31, s[36:39], s72 offen offset:1024 lds
  buffer_load_dwordx4 v32, s[36:39], s72 offen offset:2048 lds
  buffer_load_dwordx4 v33, s[36:39], s72 offen offset:3072 lds
  s_waitcnt vmcnt(13)
  ds_read_b128 a[160:163], v49 offset:32768
  ds_read_b128 a[164:167], v50 offset:32768
  ds_read_b128 a[168:171], v49 offset:34816
  ds_read_b128 a[172:175], v50 offset:34816
  s_waitcnt vmcnt(8)
  v_cmp_ne_u64 vcc, s[66:67], v[20:21]
  s_cmp_lg_u64 vcc, 0
  s_cbranch_scc1 L9_%=
L10_%=:
  s_add_u32 s51, s51, 0x8000
  s_add_u32 s77, s78, 0
  buffer_load_dwordx4 v[52:55], v2, s[8:11], s77 offen sc1
  buffer_load_dwordx4 v[56:59], v2, s[8:11], s77 offen offset:1024 sc1
  buffer_load_dwordx4 v[60:63], v2, s[8:11], s77 offen offset:2048 sc1
  buffer_load_dwordx4 v[72:75], v2, s[8:11], s77 offen offset:3072 sc1
  s_add_u32 s77, s78, 0x1000
  buffer_load_dwordx4 v[76:79], v2, s[8:11], s77 offen sc1
  buffer_load_dwordx4 v[106:109], v2, s[8:11], s77 offen offset:1024 sc1
  buffer_load_dwordx4 v[110:113], v2, s[8:11], s77 offen offset:2048 sc1
  buffer_load_dwordx4 v[114:117], v2, s[8:11], s77 offen offset:3072 sc1
  s_add_u32 s77, s79, 0
  buffer_load_dwordx4 v[118:121], v2, s[8:11], s77 offen sc1
  ds_read_b128 a[176:179], v49 offset:12288
  ds_read_b128 a[180:183], v50 offset:12288
  ds_read_b128 a[184:187], v49 offset:14336
  ds_read_b128 a[188:191], v50 offset:14336
  v_permlane16_swap_b32 v98, v100
  v_permlane16_swap_b32 v99, v101
  v_permlane16_swap_b32 v102, v104
  v_permlane16_swap_b32 v103, v105
  v_lshlrev_b32 v176, 16, v98
  v_and_b32 v177, 0xffff0000, v98
  v_lshlrev_b32 v178, 16, v99
  v_and_b32 v179, 0xffff0000, v99
  v_lshlrev_b32 v180, 16, v100
  v_and_b32 v181, 0xffff0000, v100
  v_lshlrev_b32 v182, 16, v101
  v_and_b32 v183, 0xffff0000, v101
  v_lshlrev_b32 v184, 16, v102
  v_and_b32 v185, 0xffff0000, v102
  v_lshlrev_b32 v186, 16, v103
  v_and_b32 v187, 0xffff0000, v103
LS14_%=:
  s_cbranch_execz L3_%=
  v_lshlrev_b32 v188, 16, v104
  v_and_b32 v189, 0xffff0000, v104
  v_lshlrev_b32 v190, 16, v105
  v_and_b32 v191, 0xffff0000, v105
  s_waitcnt vmcnt(8)
  v_mfma_f32_16x16x32_bf16 v[192:195], a[192:195], v[52:55], 0
  buffer_load_dwordx4 v[122:125], v2, s[8:11], s77 offen offset:1024 sc1
  v_mfma_f32_16x16x32_bf16 v[196:199], a[200:203], v[52:55], 0
  v_mfma_f32_16x16x32_bf16 v[200:203], a[208:211], v[52:55], 0
  buffer_load_dwordx4 v[126:129], v2, s[8:11], s77 offen offset:2048 sc1
  v_mfma_f32_16x16x32_bf16 v[204:207], a[216:219], v[52:55], 0
  s_waitcnt vmcnt(9)
  v_mfma_f32_16x16x32_bf16 v[192:195], a[196:199], v[56:59], v[192:195]
  buffer_load_dwordx4 v[130:133], v2, s[8:11], s77 offen offset:3072 sc1
  v_mfma_f32_16x16x32_bf16 v[196:199], a[204:207], v[56:59], v[196:199]
  v_mfma_f32_16x16x32_bf16 v[200:203], a[212:215], v[56:59], v[200:203]
  v_mfma_f32_16x16x32_bf16 v[204:207], a[220:223], v[56:59], v[204:207]
  s_waitcnt vmcnt(9)
  v_mfma_f32_16x16x32_bf16 v[192:195], a[224:227], v[60:63], v[192:195]
  s_add_u32 s77, s79, 0x1000
  s_waitcnt vmcnt(8)
  v_mfma_f32_16x16x32_bf16 v[192:195], a[228:231], v[72:75], v[192:195]
  s_waitcnt vmcnt(7) lgkmcnt(14)
  v_mfma_f32_16x16x32_bf16 v[192:195], a[128:131], v[76:79], v[192:195]
  v_mfma_f32_16x16x32_bf16 v[196:199], a[232:235], v[60:63], v[196:199]
  buffer_load_dwordx4 v[134:137], v2, s[8:11], s77 offen sc1
  v_mfma_f32_16x16x32_bf16 v[196:199], a[236:239], v[72:75], v[196:199]
  s_waitcnt lgkmcnt(13)
  v_mfma_f32_16x16x32_bf16 v[196:199], a[136:139], v[76:79], v[196:199]
  v_mfma_f32_16x16x32_bf16 v[200:203], a[240:243], v[60:63], v[200:203]
  buffer_load_dwordx4 v[138:141], v2, s[8:11], s77 offen offset:1024 sc1
  v_mfma_f32_16x16x32_bf16 v[200:203], a[244:247], v[72:75], v[200:203]
  s_waitcnt lgkmcnt(11)
  v_mfma_f32_16x16x32_bf16 v[200:203], a[144:147], v[76:79], v[200:203]
  v_mfma_f32_16x16x32_bf16 v[204:207], a[248:251], v[60:63], v[204:207]
  buffer_load_dwordx4 v[142:145], v2, s[8:11], s77 offen offset:2048 sc1
  v_mfma_f32_16x16x32_bf16 v[204:207], a[252:255], v[72:75], v[204:207]
  s_waitcnt lgkmcnt(9)
  v_mfma_f32_16x16x32_bf16 v[204:207], a[152:155], v[76:79], v[204:207]
  s_waitcnt vmcnt(9)
  v_mfma_f32_16x16x32_bf16 v[192:195], a[132:135], v[106:109], v[192:195]
  buffer_load_dwordx4 v[146:149], v2, s[8:11], s77 offen offset:3072 sc1
  s_waitcnt vmcnt(9) lgkmcnt(7)
  v_mfma_f32_16x16x32_bf16 v[192:195], a[160:163], v[110:113], v[192:195]
  s_waitcnt vmcnt(8) lgkmcnt(6)
  v_mfma_f32_16x16x32_bf16 v[192:195], a[164:167], v[114:117], v[192:195]
  v_mfma_f32_16x16x32_bf16 v[196:199], a[140:143], v[106:109], v[196:199]
  s_add_u32 s77, s78, 0x10000
  s_waitcnt lgkmcnt(5)
  v_mfma_f32_16x16x32_bf16 v[196:199], a[168:171], v[110:113], v[196:199]
  s_waitcnt lgkmcnt(4)
  v_mfma_f32_16x16x32_bf16 v[196:199], a[172:175], v[114:117], v[196:199]
  v_mfma_f32_16x16x32_bf16 v[200:203], a[148:151], v[106:109], v[200:203]
  buffer_load_dwordx4 v[52:55], v2, s[8:11], s77 offen sc1
  s_waitcnt lgkmcnt(3)
  v_mfma_f32_16x16x32_bf16 v[200:203], a[176:179], v[110:113], v[200:203]
  s_waitcnt lgkmcnt(2)
  v_mfma_f32_16x16x32_bf16 v[200:203], a[180:183], v[114:117], v[200:203]
  v_mfma_f32_16x16x32_bf16 v[204:207], a[156:159], v[106:109], v[204:207]
  buffer_load_dwordx4 v[56:59], v2, s[8:11], s77 offen offset:1024 sc1
  s_waitcnt lgkmcnt(1)
  v_mfma_f32_16x16x32_bf16 v[204:207], a[184:187], v[110:113], v[204:207]
  s_waitcnt lgkmcnt(0)
  v_mfma_f32_16x16x32_bf16 v[204:207], a[188:191], v[114:117], v[204:207]
  s_waitcnt vmcnt(9)
  v_mfma_f32_16x16x32_bf16 v[208:211], a[192:195], v[118:121], 0
  buffer_load_dwordx4 v[60:63], v2, s[8:11], s77 offen offset:2048 sc1
  s_waitcnt vmcnt(9)
  v_mfma_f32_16x16x32_bf16 v[208:211], a[196:199], v[122:125], v[208:211]
  s_waitcnt vmcnt(8)
  v_mfma_f32_16x16x32_bf16 v[208:211], a[224:227], v[126:129], v[208:211]
  s_waitcnt vmcnt(7)
  v_mfma_f32_16x16x32_bf16 v[208:211], a[228:231], v[130:133], v[208:211]
  buffer_load_dwordx4 v[72:75], v2, s[8:11], s77 offen offset:3072 sc1
  v_mfma_f32_16x16x32_bf16 v[212:215], a[200:203], v[118:121], 0
  ds_write_b128 v11, v[192:195]
  v_mfma_f32_16x16x32_bf16 v[212:215], a[204:207], v[122:125], v[212:215]
  v_mfma_f32_16x16x32_bf16 v[212:215], a[232:235], v[126:129], v[212:215]
  s_add_u32 s77, s78, 0x11000
  v_mfma_f32_16x16x32_bf16 v[212:215], a[236:239], v[130:133], v[212:215]
  ds_write_b128 v11, v[196:199] offset:1024
  v_mfma_f32_16x16x32_bf16 v[216:219], a[208:211], v[118:121], 0
  v_mfma_f32_16x16x32_bf16 v[216:219], a[212:215], v[122:125], v[216:219]
  buffer_load_dwordx4 v[76:79], v2, s[8:11], s77 offen sc1
  v_mfma_f32_16x16x32_bf16 v[216:219], a[240:243], v[126:129], v[216:219]
  ds_write_b128 v11, v[200:203] offset:2048
  v_mfma_f32_16x16x32_bf16 v[216:219], a[244:247], v[130:133], v[216:219]
  v_mfma_f32_16x16x32_bf16 v[220:223], a[216:219], v[118:121], 0
  buffer_load_dwordx4 v[106:109], v2, s[8:11], s77 offen offset:1024 sc1
  v_mfma_f32_16x16x32_bf16 v[220:223], a[220:223], v[122:125], v[220:223]
  ds_write_b128 v11, v[204:207] offset:3072
  v_mfma_f32_16x16x32_bf16 v[220:223], a[248:251], v[126:129], v[220:223]
  v_mfma_f32_16x16x32_bf16 v[220:223], a[252:255], v[130:133], v[220:223]
  s_waitcnt vmcnt(9)
  v_mfma_f32_16x16x32_bf16 v[208:211], a[128:131], v[134:137], v[208:211]
  buffer_load_dwordx4 v[110:113], v2, s[8:11], s77 offen offset:2048 sc1
  s_waitcnt vmcnt(9)
  v_mfma_f32_16x16x32_bf16 v[208:211], a[132:135], v[138:141], v[208:211]
  s_waitcnt vmcnt(8)
  v_mfma_f32_16x16x32_bf16 v[208:211], a[160:163], v[142:145], v[208:211]
  s_waitcnt vmcnt(7)
  v_mfma_f32_16x16x32_bf16 v[208:211], a[164:167], v[146:149], v[208:211]
  buffer_load_dwordx4 v[114:117], v2, s[8:11], s77 offen offset:3072 sc1
  v_mfma_f32_16x16x32_bf16 v[212:215], a[136:139], v[134:137], v[212:215]
  v_mfma_f32_16x16x32_bf16 v[212:215], a[140:143], v[138:141], v[212:215]
  v_mfma_f32_16x16x32_bf16 v[212:215], a[168:171], v[142:145], v[212:215]
  s_add_u32 s77, s79, 0x10000
  v_mfma_f32_16x16x32_bf16 v[212:215], a[172:175], v[146:149], v[212:215]
  v_mfma_f32_16x16x32_bf16 v[216:219], a[144:147], v[134:137], v[216:219]
  v_mfma_f32_16x16x32_bf16 v[216:219], a[148:151], v[138:141], v[216:219]
  buffer_load_dwordx4 v[118:121], v2, s[8:11], s77 offen sc1
  v_mfma_f32_16x16x32_bf16 v[216:219], a[176:179], v[142:145], v[216:219]
  v_mfma_f32_16x16x32_bf16 v[216:219], a[180:183], v[146:149], v[216:219]
  v_mfma_f32_16x16x32_bf16 v[220:223], a[152:155], v[134:137], v[220:223]
  buffer_load_dwordx4 v[122:125], v2, s[8:11], s77 offen offset:1024 sc1
  v_mfma_f32_16x16x32_bf16 v[220:223], a[156:159], v[138:141], v[220:223]
  v_mfma_f32_16x16x32_bf16 v[220:223], a[184:187], v[142:145], v[220:223]
  v_mfma_f32_16x16x32_bf16 v[220:223], a[188:191], v[146:149], v[220:223]
  s_waitcnt vmcnt(9)
  v_mfma_f32_16x16x32_bf16 v[224:227], a[192:195], v[52:55], 0
  buffer_load_dwordx4 v[126:129], v2, s[8:11], s77 offen offset:2048 sc1
  s_waitcnt vmcnt(9)
  v_mfma_f32_16x16x32_bf16 v[224:227], a[196:199], v[56:59], v[224:227]
  s_waitcnt vmcnt(8)
  v_mfma_f32_16x16x32_bf16 v[224:227], a[224:227], v[60:63], v[224:227]
  s_waitcnt vmcnt(7)
  v_mfma_f32_16x16x32_bf16 v[224:227], a[228:231], v[72:75], v[224:227]
  buffer_load_dwordx4 v[130:133], v2, s[8:11], s77 offen offset:3072 sc1
  v_mfma_f32_16x16x32_bf16 v[228:231], a[200:203], v[52:55], 0
  ds_write_b128 v11, v[208:211] offset:4096
  v_mfma_f32_16x16x32_bf16 v[228:231], a[204:207], v[56:59], v[228:231]
  ds_write_b128 v11, v[212:215] offset:5120
  v_mfma_f32_16x16x32_bf16 v[228:231], a[232:235], v[60:63], v[228:231]
  s_add_u32 s77, s79, 0x11000
  v_mfma_f32_16x16x32_bf16 v[228:231], a[236:239], v[72:75], v[228:231]
  v_mfma_f32_16x16x32_bf16 v[232:235], a[208:211], v[52:55], 0
  v_mfma_f32_16x16x32_bf16 v[232:235], a[212:215], v[56:59], v[232:235]
  buffer_load_dwordx4 v[134:137], v2, s[8:11], s77 offen sc1
  v_mfma_f32_16x16x32_bf16 v[232:235], a[240:243], v[60:63], v[232:235]
  v_mfma_f32_16x16x32_bf16 v[232:235], a[244:247], v[72:75], v[232:235]
  v_mfma_f32_16x16x32_bf16 v[236:239], a[216:219], v[52:55], 0
  buffer_load_dwordx4 v[138:141], v2, s[8:11], s77 offen offset:1024 sc1
  v_mfma_f32_16x16x32_bf16 v[236:239], a[220:223], v[56:59], v[236:239]
LS15_%=:
  s_cbranch_execz L3_%=
  v_mfma_f32_16x16x32_bf16 v[236:239], a[248:251], v[60:63], v[236:239]
  s_waitcnt lgkmcnt(0)
  s_barrier
  v_mfma_f32_16x16x32_bf16 v[236:239], a[252:255], v[72:75], v[236:239]
  s_waitcnt vmcnt(9)
  v_mfma_f32_16x16x32_bf16 v[224:227], a[128:131], v[76:79], v[224:227]
  buffer_load_dwordx4 v[142:145], v2, s[8:11], s77 offen offset:2048 sc1
  s_waitcnt vmcnt(9)
  v_mfma_f32_16x16x32_bf16 v[224:227], a[132:135], v[106:109], v[224:227]
  ds_read_b128 v[192:195], v46
  s_waitcnt vmcnt(8)
  v_mfma_f32_16x16x32_bf16 v[224:227], a[160:163], v[110:113], v[224:227]
  ds_read_b128 v[196:199], v46 offset:1024
  s_waitcnt vmcnt(7)
  v_mfma_f32_16x16x32_bf16 v[224:227], a[164:167], v[114:117], v[224:227]
  ds_read_b128 v[200:203], v47 offset:2048
  v_mfma_f32_16x16x32_bf16 v[228:231], a[136:139], v[76:79], v[228:231]
  ds_read_b128 v[204:207], v47 offset:3072
  v_mfma_f32_16x16x32_bf16 v[228:231], a[140:143], v[106:109], v[228:231]
  buffer_load_dwordx4 v[146:149], v2, s[8:11], s77 offen offset:3072 sc1
  v_mfma_f32_16x16x32_bf16 v[228:231], a[168:171], v[110:113], v[228:231]
  ds_read_b128 v[208:211], v48 offset:4096
  v_mfma_f32_16x16x32_bf16 v[228:231], a[172:175], v[114:117], v[228:231]
  ds_read_b128 v[212:215], v48 offset:5120
  s_waitcnt lgkmcnt(5)
  v_add_f32 v216, v216, v192
  v_mfma_f32_16x16x32_bf16 v[232:235], a[144:147], v[76:79], v[232:235]
  v_add_f32 v217, v217, v193
  v_add_f32 v218, v218, v194
  v_mfma_f32_16x16x32_bf16 v[232:235], a[148:151], v[106:109], v[232:235]
  v_add_f32 v219, v219, v195
  s_waitcnt lgkmcnt(4)
  v_add_f32 v220, v220, v196
  v_mfma_f32_16x16x32_bf16 v[232:235], a[176:179], v[110:113], v[232:235]
  s_and_b32 s52, s58, 1
  s_lshl_b32 s52, s52, 10
  s_add_u32 m0, s52, 0x26400
  s_nop 0
  buffer_load_dwordx4 v2, s[8:11], s81 offen sc1 lds
  v_mfma_f32_16x16x32_bf16 v[232:235], a[180:183], v[114:117], v[232:235]
  s_add_u32 m0, s63, 0x1000
  s_add_u32 s72, s72, 0x1c000
  v_add_f32 v221, v221, v197
  v_mfma_f32_16x16x32_bf16 v[236:239], a[152:155], v[76:79], v[236:239]
  buffer_load_dwordx4 v30, s[36:39], s72 offen lds
  v_add_f32 v222, v222, v198
  v_mfma_f32_16x16x32_bf16 v[236:239], a[156:159], v[106:109], v[236:239]
  buffer_load_dwordx4 v31, s[36:39], s72 offen offset:1024 lds
  v_add_f32 v223, v223, v199
  s_waitcnt lgkmcnt(3)
  v_add_f32 v216, v216, v200
  v_add_f32 v217, v217, v201
  v_mfma_f32_16x16x32_bf16 v[236:239], a[184:187], v[110:113], v[236:239]
  ds_read_b64_tr_b16 a[0:1], v38 offset:28672
  ds_read_b64_tr_b16 a[2:3], v38 offset:30720
  v_add_f32 v218, v218, v202
  v_add_f32 v219, v219, v203
  s_waitcnt lgkmcnt(4)
  v_add_f32 v220, v220, v204
  v_mfma_f32_16x16x32_bf16 v[236:239], a[188:191], v[114:117], v[236:239]
  ds_read_b64_tr_b16 a[4:5], v39 offset:28672
  ds_read_b64_tr_b16 a[6:7], v39 offset:30720
  v_add_f32 v221, v221, v205
  v_add_f32 v222, v222, v206
  v_add_f32 v223, v223, v207
  s_waitcnt vmcnt(10)
  v_mfma_f32_16x16x32_bf16 v[240:243], a[192:195], v[118:121], 0
  buffer_load_dwordx4 v32, s[36:39], s72 offen offset:2048 lds
  s_waitcnt vmcnt(10)
  v_mfma_f32_16x16x32_bf16 v[240:243], a[196:199], v[122:125], v[240:243]
  ds_read_b64_tr_b16 a[8:9], v40 offset:28672
  ds_read_b64_tr_b16 a[10:11], v40 offset:30720
  s_waitcnt lgkmcnt(7)
  v_add_f32 v216, v216, v208
  s_waitcnt vmcnt(9)
  v_mfma_f32_16x16x32_bf16 v[240:243], a[224:227], v[126:129], v[240:243]
  ds_read_b64_tr_b16 a[12:13], v41 offset:28672
  ds_read_b64_tr_b16 a[14:15], v41 offset:30720
  v_add_f32 v217, v217, v209
  s_waitcnt vmcnt(8)
  v_mfma_f32_16x16x32_bf16 v[240:243], a[228:231], v[130:133], v[240:243]
  ds_write_b128 v11, v[224:227] offset:6144
  v_mfma_f32_16x16x32_bf16 v[244:247], a[200:203], v[118:121], 0
  buffer_load_dwordx4 v33, s[36:39], s72 offen offset:3072 lds
  v_mfma_f32_16x16x32_bf16 v[244:247], a[204:207], v[122:125], v[244:247]
  ds_read_b64_tr_b16 a[16:17], v38
  ds_read_b64_tr_b16 a[18:19], v38 offset:2048
  v_add_f32 v218, v218, v210
  v_mfma_f32_16x16x32_bf16 v[244:247], a[232:235], v[126:129], v[244:247]
  ds_write_b128 v11, v[228:231] offset:7168
  v_mfma_f32_16x16x32_bf16 v[244:247], a[236:239], v[130:133], v[244:247]
  ds_read_b64_tr_b16 a[20:21], v39
  ds_read_b64_tr_b16 a[22:23], v39 offset:2048
  v_add_f32 v219, v219, v211
  v_mfma_f32_16x16x32_bf16 v[248:251], a[208:211], v[118:121], 0
  ds_read_b64_tr_b16 a[24:25], v40
  ds_read_b64_tr_b16 a[26:27], v40 offset:2048
  s_waitcnt lgkmcnt(14)
  v_add_f32 v220, v220, v212
  v_mfma_f32_16x16x32_bf16 v[248:251], a[212:215], v[122:125], v[248:251]
  ds_write_b128 v11, v[232:235] offset:8192
  v_mfma_f32_16x16x32_bf16 v[248:251], a[240:243], v[126:129], v[248:251]
  ds_read_b64_tr_b16 a[28:29], v41
  ds_read_b64_tr_b16 a[30:31], v41 offset:2048
  s_add_u32 m0, s63, 0x2000
  v_add_f32 v221, v221, v213
  v_add_f32 v222, v222, v214
  v_mfma_f32_16x16x32_bf16 v[248:251], a[244:247], v[130:133], v[248:251]
  s_add_u32 s72, s72, 0x1c000
  v_add_f32 v223, v223, v215
  v_fmac_f32 v176, v216, v82
  v_fmac_f32 v177, v217, v83
  v_mfma_f32_16x16x32_bf16 v[252:255], a[216:219], v[118:121], 0
  ds_write_b128 v11, v[236:239] offset:9216
  v_fmac_f32 v178, v218, v84
  v_fmac_f32 v179, v219, v85
  v_fmac_f32 v180, v220, v86
  v_mfma_f32_16x16x32_bf16 v[252:255], a[220:223], v[122:125], v[252:255]
  buffer_load_dwordx4 v30, s[36:39], s72 offen lds
  v_fmac_f32 v181, v221, v87
  v_fmac_f32 v182, v222, v88
  v_fmac_f32 v183, v223, v89
  v_mfma_f32_16x16x32_bf16 v[252:255], a[248:251], v[126:129], v[252:255]
  buffer_load_dwordx4 v31, s[36:39], s72 offen offset:1024 lds
  v_mul_f32 v192, v176, v90
  v_mul_f32 v193, v177, v91
  v_mul_f32 v194, v178, v92
  v_mfma_f32_16x16x32_bf16 v[252:255], a[252:255], v[130:133], v[252:255]
  v_mul_f32 v195, v179, v93
  v_mul_f32 v196, v180, v94
  v_mul_f32 v197, v181, v95
  s_waitcnt vmcnt(10)
  v_mfma_f32_16x16x32_bf16 v[240:243], a[128:131], v[134:137], v[240:243]
  v_mul_f32 v198, v182, v96
  v_mul_f32 v199, v183, v97
  s_waitcnt vmcnt(9)
  v_mfma_f32_16x16x32_bf16 v[240:243], a[132:135], v[138:141], v[240:243]
  buffer_load_dwordx4 v32, s[36:39], s72 offen offset:2048 lds
  s_waitcnt vmcnt(9)
  v_mfma_f32_16x16x32_bf16 v[240:243], a[160:163], v[142:145], v[240:243]
  v_cvt_pk_bf16_f32 v200, v192, v193
  v_cvt_pk_bf16_f32 v201, v194, v195
  s_waitcnt vmcnt(8)
  v_mfma_f32_16x16x32_bf16 v[240:243], a[164:167], v[146:149], v[240:243]
  v_cvt_pk_bf16_f32 v202, v196, v197
  v_cvt_pk_bf16_f32 v203, v198, v199
  v_mfma_f32_16x16x32_bf16 v[244:247], a[136:139], v[134:137], v[244:247]
  buffer_store_dwordx4 v[200:203], v2, s[8:11], s82 offen sc1
  v_mfma_f32_16x16x32_bf16 v[244:247], a[140:143], v[138:141], v[244:247]
  buffer_load_dwordx4 v33, s[36:39], s72 offen offset:3072 lds
  v_mfma_f32_16x16x32_bf16 v[244:247], a[168:171], v[142:145], v[244:247]
  v_add_f32 v150, v176, v177
  v_mul_f32 v151, v176, v176
  v_mfma_f32_16x16x32_bf16 v[244:247], a[172:175], v[146:149], v[244:247]
  v_fmac_f32 v151, v177, v177
  v_add_f32 v150, v150, v178
  v_mfma_f32_16x16x32_bf16 v[248:251], a[144:147], v[134:137], v[248:251]
  v_fmac_f32 v151, v178, v178
  v_add_f32 v150, v150, v179
  v_mfma_f32_16x16x32_bf16 v[248:251], a[148:151], v[138:141], v[248:251]
  v_fmac_f32 v151, v179, v179
  s_add_u32 m0, s63, 0x8000
  v_mfma_f32_16x16x32_bf16 v[248:251], a[176:179], v[142:145], v[248:251]
LS16_%=:
  s_cbranch_execz LS20_%=
  s_add_u32 s72, s72, 0x1c000
  v_add_f32 v150, v150, v180
  v_fmac_f32 v151, v180, v180
  v_mfma_f32_16x16x32_bf16 v[248:251], a[180:183], v[146:149], v[248:251]
  ds_write_b128 v11, v[240:243] offset:10240
  v_mfma_f32_16x16x32_bf16 v[252:255], a[152:155], v[134:137], v[252:255]
  buffer_load_dwordx4 v30, s[36:39], s72 offen lds
  v_mfma_f32_16x16x32_bf16 v[252:255], a[156:159], v[138:141], v[252:255]
  ds_write_b128 v11, v[244:247] offset:11264
  v_mfma_f32_16x16x32_bf16 v[252:255], a[184:187], v[142:145], v[252:255]
  buffer_load_dwordx4 v31, s[36:39], s72 offen offset:1024 lds
  v_add_f32 v150, v150, v181
  v_fmac_f32 v151, v181, v181
  v_add_f32 v150, v150, v182
  v_mfma_f32_16x16x32_bf16 v[252:255], a[188:191], v[146:149], v[252:255]
  v_fmac_f32 v151, v182, v182
  v_add_f32 v150, v150, v183
  v_fmac_f32 v151, v183, v183
  s_waitcnt vmcnt(11)
  s_waitcnt lgkmcnt(0)
  s_barrier
  ds_read_b128 v[224:227], v46 offset:6144
  ds_read_b128 v[228:231], v46 offset:7168
  ds_read_b128 v[232:235], v47 offset:8192
  ds_read_b128 v[236:239], v47 offset:9216
  ds_read_b128 v[240:243], v48 offset:10240
  ds_read_b128 v[244:247], v48 offset:11264
  s_waitcnt lgkmcnt(5)
  v_pk_add_f32 v[248:249], v[248:249], v[224:225]
  buffer_load_dwordx4 v32, s[36:39], s72 offen offset:2048 lds
  v_pk_add_f32 v[250:251], v[250:251], v[226:227]
  buffer_load_dwordx4 v33, s[36:39], s72 offen offset:3072 lds
  s_waitcnt lgkmcnt(4)
  v_pk_add_f32 v[252:253], v[252:253], v[228:229]
  v_pk_add_f32 v[254:255], v[254:255], v[230:231]
  s_waitcnt lgkmcnt(3)
  v_pk_add_f32 v[248:249], v[248:249], v[232:233]
  v_pk_add_f32 v[250:251], v[250:251], v[234:235]
  s_waitcnt lgkmcnt(2)
  v_pk_add_f32 v[252:253], v[252:253], v[236:237]
  s_add_u32 m0, s63, 0x3000
  v_pk_add_f32 v[254:255], v[254:255], v[238:239]
  s_add_u32 s72, s72, 0x1c000
  s_waitcnt lgkmcnt(1)
  v_pk_add_f32 v[248:249], v[248:249], v[240:241]
  buffer_load_dwordx4 v30, s[36:39], s72 offen lds
  v_pk_add_f32 v[250:251], v[250:251], v[242:243]
  s_waitcnt lgkmcnt(0)
  v_pk_add_f32 v[252:253], v[252:253], v[244:245]
  v_pk_add_f32 v[254:255], v[254:255], v[246:247]
  v_pk_fma_f32 v[184:185], v[248:249], v[82:83], v[184:185]
  v_pk_fma_f32 v[186:187], v[250:251], v[84:85], v[186:187]
  v_pk_fma_f32 v[188:189], v[252:253], v[86:87], v[188:189]
  v_pk_fma_f32 v[190:191], v[254:255], v[88:89], v[190:191]
  v_pk_mul_f32 v[224:225], v[184:185], v[90:91]
  v_pk_mul_f32 v[226:227], v[186:187], v[92:93]
  v_pk_mul_f32 v[228:229], v[188:189], v[94:95]
  buffer_load_dwordx4 v31, s[36:39], s72 offen offset:1024 lds
  v_pk_mul_f32 v[230:231], v[190:191], v[96:97]
  v_cvt_pk_bf16_f32 v232, v224, v225
  v_cvt_pk_bf16_f32 v233, v226, v227
  v_cvt_pk_bf16_f32 v234, v228, v229
  v_cvt_pk_bf16_f32 v235, v230, v231
  buffer_store_dwordx4 v[232:235], v2, s[8:11], s83 offen sc1
  v_pk_add_f32 v[154:155], v[184:185], v[186:187]
  v_pk_mul_f32 v[156:157], v[184:185], v[184:185]
  v_pk_fma_f32 v[156:157], v[186:187], v[186:187], v[156:157]
  v_pk_add_f32 v[154:155], v[154:155], v[188:189]
  v_pk_fma_f32 v[156:157], v[188:189], v[188:189], v[156:157]
  v_pk_add_f32 v[154:155], v[154:155], v[190:191]
  v_pk_fma_f32 v[156:157], v[190:191], v[190:191], v[156:157]
  buffer_load_dwordx4 v32, s[36:39], s72 offen offset:2048 lds
  v_add_f32 v154, v154, v155
  v_add_f32 v155, v156, v157
  s_nop 0
  v_permlane32_swap_b32 v150, v154
  v_permlane32_swap_b32 v151, v155
  v_pk_add_f32 v[150:151], v[150:151], v[154:155]
  s_nop 1
  v_permlane16_swap_b32 v150, v151
  v_add_f32 v150, v150, v151
  buffer_store_dword v150, v64, s[8:11], s80 offen sc1
  buffer_load_dwordx4 v33, s[36:39], s72 offen offset:3072 lds
  s_add_u32 m0, s63, 0
  s_add_u32 s72, s72, 0x1c000
  buffer_load_dwordx4 v30, s[36:39], s72 offen lds
  buffer_load_dwordx4 v31, s[36:39], s72 offen offset:1024 lds
  buffer_load_dwordx4 v32, s[36:39], s72 offen offset:2048 lds
  buffer_load_dwordx4 v33, s[36:39], s72 offen offset:3072 lds
  s_waitcnt vmcnt(19)
  ds_read_b64_tr_b16 a[32:33], v38 offset:4096
  ds_read_b64_tr_b16 a[34:35], v38 offset:6144
  ds_read_b64_tr_b16 a[36:37], v39 offset:4096
  ds_read_b64_tr_b16 a[38:39], v39 offset:6144
  ds_read_b64_tr_b16 a[40:41], v40 offset:4096
  ds_read_b64_tr_b16 a[42:43], v40 offset:6144
  ds_read_b64_tr_b16 a[44:45], v41 offset:4096
  ds_read_b64_tr_b16 a[46:47], v41 offset:6144
  s_add_u32 m0, s63, 0x1000
  s_add_u32 s72, s72, 0x1c000
  s_waitcnt lgkmcnt(0)
  buffer_load_dwordx4 v30, s[36:39], s72 offen lds
  buffer_load_dwordx4 v31, s[36:39], s72 offen offset:1024 lds
  buffer_load_dwordx4 v32, s[36:39], s72 offen offset:2048 lds
  buffer_load_dwordx4 v33, s[36:39], s72 offen offset:3072 lds
  ds_read_b128 v[70:73], v14
  ds_read_b128 v[74:77], v14 offset:64
  s_waitcnt lgkmcnt(1)
  v_mfma_f32_16x16x32_bf16 v[52:55], v[70:73], a[0:3], 0
  v_mfma_f32_16x16x32_bf16 v[56:59], v[70:73], a[4:7], 0
  v_mfma_f32_16x16x32_bf16 v[60:63], v[70:73], a[8:11], 0
  v_mfma_f32_16x16x32_bf16 v[66:69], v[70:73], a[12:15], 0
  ds_read_b128 v[70:73], v14 offset:128
  s_waitcnt lgkmcnt(1)
  v_mfma_f32_16x16x32_bf16 v[52:55], v[74:77], a[16:19], v[52:55]
  v_mfma_f32_16x16x32_bf16 v[56:59], v[74:77], a[20:23], v[56:59]
  v_mfma_f32_16x16x32_bf16 v[60:63], v[74:77], a[24:27], v[60:63]
  v_mfma_f32_16x16x32_bf16 v[66:69], v[74:77], a[28:31], v[66:69]
  ds_read_b128 v[74:77], v14 offset:192
  s_waitcnt lgkmcnt(1)
  v_mfma_f32_16x16x32_bf16 v[52:55], v[70:73], a[32:35], v[52:55]
  v_mfma_f32_16x16x32_bf16 v[56:59], v[70:73], a[36:39], v[56:59]
  v_mfma_f32_16x16x32_bf16 v[60:63], v[70:73], a[40:43], v[60:63]
  v_mfma_f32_16x16x32_bf16 v[66:69], v[70:73], a[44:47], v[66:69]
  s_waitcnt vmcnt(9)
  s_barrier
  buffer_store_dwordx2 v[18:19], v9, s[8:11], s50 offen sc1
  s_add_u32 s50, s50, 0x8000
  s_lshr_b32 s52, s40, 7
  s_sub_u32 s52, 3, s52
  s_and_b32 s52, s52, 7
  s_xor_b32 s54, s52, s13
  s_cmp_lt_u32 s54, 4
  s_cselect_b32 s54, 0, 0x40000000
  s_lshl_b32 s52, s52, 7
  s_lshr_b32 s53, s2, 3
  s_lshl2_add_u32 s53, s53, s58
  s_mul_i32 s53, s53, 0x3800
  s_add_u32 s52, s52, s53
  v_lshrrev_b32 v3, 2, v1
  v_min_u32 v3, 13, v3
  v_lshl_add_u32 v3, v3, 10, s54
  buffer_load_dword v8, v3, s[40:43], s52 offen
  s_lshl_b32 s52, s12, 16
  s_lshl_b32 s53, s58, 12
  s_add_u32 s52, s52, s53
  s_add_u32 s4, s52, 0x700000
  s_lshl_b32 s52, s12, 13
  s_lshl_b32 s53, s58, 11
  s_add_u32 s52, s52, s53
  s_add_u32 s80, s52, 0x900000
  v_add_u32 v49, 0x4000, v2
  v_add_u32 v50, 0x16000, v2
LS17_%=:
  s_cbranch_execz LS21_%=
  s_mul_i32 s52, s12, 0x38000
  s_add_u32 s52, s52, 0xa00000
  s_lshl2_add_u32 s53, s13, s58
  s_lshl_b32 s53, s53, 11
  s_add_u32 s18, s52, s53
  s_add_u32 s19, s18, 0x1c000
  s_mul_i32 s53, s58, 0x3800
  s_add_u32 s52, s52, s53
  s_xor_b32 s53, s76, 1
  s_mul_i32 s54, s53, 0xe000
  s_add_u32 s78, s52, s54
  s_mul_i32 s54, s76, 0xe000
  s_add_u32 s79, s52, s54
  s_mov_b32 s6, -1
  s_mov_b32 s7, 0
  ds_read_b64_tr_b16 a[48:49], v38 offset:8192
  ds_read_b64_tr_b16 a[50:51], v38 offset:10240
  ds_read_b64_tr_b16 a[52:53], v39 offset:8192
  ds_read_b64_tr_b16 a[54:55], v39 offset:10240
  ds_read_b64_tr_b16 a[56:57], v40 offset:8192
  ds_read_b64_tr_b16 a[58:59], v40 offset:10240
  ds_read_b64_tr_b16 a[60:61], v41 offset:8192
  ds_read_b64_tr_b16 a[62:63], v41 offset:10240
  s_add_u32 m0, s63, 0x2000
  s_add_u32 s72, s72, 0x1c000
  s_waitcnt lgkmcnt(0)
  buffer_load_dwordx4 v30, s[36:39], s72 offen lds
  buffer_load_dwordx4 v31, s[36:39], s72 offen offset:1024 lds
  buffer_load_dwordx4 v32, s[36:39], s72 offen offset:2048 lds
  buffer_load_dwordx4 v33, s[36:39], s72 offen offset:3072 lds
  ds_read_b128 v[70:73], v14 offset:256
  v_mfma_f32_16x16x32_bf16 v[52:55], v[74:77], a[48:51], v[52:55]
  v_mfma_f32_16x16x32_bf16 v[56:59], v[74:77], a[52:55], v[56:59]
  v_mfma_f32_16x16x32_bf16 v[60:63], v[74:77], a[56:59], v[60:63]
  v_mfma_f32_16x16x32_bf16 v[66:69], v[74:77], a[60:63], v[66:69]
  ds_read_b64_tr_b16 a[64:65], v38 offset:32768
  ds_read_b64_tr_b16 a[66:67], v38 offset:34816
  ds_read_b64_tr_b16 a[68:69], v39 offset:32768
  ds_read_b64_tr_b16 a[70:71], v39 offset:34816
  ds_read_b64_tr_b16 a[72:73], v40 offset:32768
  ds_read_b64_tr_b16 a[74:75], v40 offset:34816
  ds_read_b64_tr_b16 a[76:77], v41 offset:32768
  ds_read_b64_tr_b16 a[78:79], v41 offset:34816
  s_waitcnt vmcnt(14)
  ds_read_b64_tr_b16 a[80:81], v38 offset:12288
  ds_read_b64_tr_b16 a[82:83], v38 offset:14336
  ds_read_b64_tr_b16 a[84:85], v39 offset:12288
  ds_read_b64_tr_b16 a[86:87], v39 offset:14336
  ds_read_b64_tr_b16 a[88:89], v40 offset:12288
  ds_read_b64_tr_b16 a[90:91], v40 offset:14336
  ds_read_b64_tr_b16 a[92:93], v41 offset:12288
  ds_read_b64_tr_b16 a[94:95], v41 offset:14336
  s_waitcnt vmcnt(10)
  ds_read_b64_tr_b16 a[96:97], v38
  ds_read_b64_tr_b16 a[98:99], v38 offset:2048
  ds_read_b64_tr_b16 a[100:101], v39
  ds_read_b64_tr_b16 a[102:103], v39 offset:2048
  ds_read_b64_tr_b16 a[104:105], v40
  ds_read_b64_tr_b16 a[106:107], v40 offset:2048
  ds_read_b64_tr_b16 a[108:109], v41
  ds_read_b64_tr_b16 a[110:111], v41 offset:2048
  s_waitcnt vmcnt(6)
  ds_read_b64_tr_b16 a[112:113], v38 offset:4096
  ds_read_b64_tr_b16 a[114:115], v38 offset:6144
  ds_read_b64_tr_b16 a[116:117], v39 offset:4096
  ds_read_b64_tr_b16 a[118:119], v39 offset:6144
  ds_read_b64_tr_b16 a[120:121], v40 offset:4096
  ds_read_b64_tr_b16 a[122:123], v40 offset:6144
  ds_read_b64_tr_b16 a[124:125], v41 offset:4096
  ds_read_b64_tr_b16 a[126:127], v41 offset:6144
  s_add_u32 m0, s63, 0x8000
  s_add_u32 s72, s72, 0x1c000
  s_waitcnt lgkmcnt(14)
  buffer_load_dwordx4 v30, s[36:39], s72 offen lds
  buffer_load_dwordx4 v31, s[36:39], s72 offen offset:1024 lds
  buffer_load_dwordx4 v32, s[36:39], s72 offen offset:2048 lds
  buffer_load_dwordx4 v33, s[36:39], s72 offen offset:3072 lds
  ds_read_b128 v[74:77], v14 offset:320
  v_mfma_f32_16x16x32_bf16 v[52:55], v[70:73], a[64:67], v[52:55]
  v_mfma_f32_16x16x32_bf16 v[56:59], v[70:73], a[68:71], v[56:59]
  v_mfma_f32_16x16x32_bf16 v[60:63], v[70:73], a[72:75], v[60:63]
  v_mfma_f32_16x16x32_bf16 v[66:69], v[70:73], a[76:79], v[66:69]
  ds_read_b128 v[70:73], v14 offset:384
  s_waitcnt lgkmcnt(1)
  v_mfma_f32_16x16x32_bf16 v[52:55], v[74:77], a[80:83], v[52:55]
  v_mfma_f32_16x16x32_bf16 v[56:59], v[74:77], a[84:87], v[56:59]
  v_mfma_f32_16x16x32_bf16 v[60:63], v[74:77], a[88:91], v[60:63]
  v_mfma_f32_16x16x32_bf16 v[66:69], v[74:77], a[92:95], v[66:69]
  s_add_u32 m0, s63, 0x3000
  s_add_u32 s72, s72, 0x1c000
  buffer_load_dwordx4 v30, s[36:39], s72 offen lds
  buffer_load_dwordx4 v31, s[36:39], s72 offen offset:1024 lds
  buffer_load_dwordx4 v32, s[36:39], s72 offen offset:2048 lds
  buffer_load_dwordx4 v33, s[36:39], s72 offen offset:3072 lds
  ds_read_b128 v[74:77], v14 offset:448
  s_waitcnt lgkmcnt(1)
  v_mfma_f32_16x16x32_bf16 v[52:55], v[70:73], a[96:99], v[52:55]
  v_mfma_f32_16x16x32_bf16 v[56:59], v[70:73], a[100:103], v[56:59]
  v_mfma_f32_16x16x32_bf16 v[60:63], v[70:73], a[104:107], v[60:63]
  v_mfma_f32_16x16x32_bf16 v[66:69], v[70:73], a[108:111], v[66:69]
  ds_read_b128 v[70:73], v14 offset:512
  s_waitcnt lgkmcnt(1)
  v_mfma_f32_16x16x32_bf16 v[52:55], v[74:77], a[112:115], v[52:55]
  v_mfma_f32_16x16x32_bf16 v[56:59], v[74:77], a[116:119], v[56:59]
  v_mfma_f32_16x16x32_bf16 v[60:63], v[74:77], a[120:123], v[60:63]
  v_mfma_f32_16x16x32_bf16 v[66:69], v[74:77], a[124:127], v[66:69]
  s_waitcnt vmcnt(8)
  ds_read_b64_tr_b16 a[128:129], v38 offset:8192
  ds_read_b64_tr_b16 a[130:131], v38 offset:10240
  ds_read_b64_tr_b16 a[132:133], v39 offset:8192
  ds_read_b64_tr_b16 a[134:135], v39 offset:10240
  ds_read_b64_tr_b16 a[136:137], v40 offset:8192
  ds_read_b64_tr_b16 a[138:139], v40 offset:10240
  ds_read_b64_tr_b16 a[140:141], v41 offset:8192
  ds_read_b64_tr_b16 a[142:143], v41 offset:10240
  s_add_u32 m0, s63, 0
  s_add_u32 s72, s72, 0x1c000
  buffer_load_dwordx4 v30, s[36:39], s72 offen lds
  buffer_load_dwordx4 v31, s[36:39], s72 offen offset:1024 lds
  buffer_load_dwordx4 v32, s[36:39], s72 offen offset:2048 lds
  buffer_load_dwordx4 v33, s[36:39], s72 offen offset:3072 lds
  ds_read_b128 v[74:77], v14 offset:576
  s_waitcnt lgkmcnt(7)
  v_mfma_f32_16x16x32_bf16 v[52:55], v[70:73], a[128:131], v[52:55]
  s_waitcnt lgkmcnt(5)
  v_mfma_f32_16x16x32_bf16 v[56:59], v[70:73], a[132:135], v[56:59]
  s_waitcnt lgkmcnt(3)
  v_mfma_f32_16x16x32_bf16 v[60:63], v[70:73], a[136:139], v[60:63]
  s_waitcnt lgkmcnt(1)
  v_mfma_f32_16x16x32_bf16 v[66:69], v[70:73], a[140:143], v[66:69]
  s_waitcnt vmcnt(8)
  ds_read_b64_tr_b16 a[144:145], v38 offset:32768
  ds_read_b64_tr_b16 a[146:147], v38 offset:34816
  ds_read_b64_tr_b16 a[148:149], v39 offset:32768
  ds_read_b64_tr_b16 a[150:151], v39 offset:34816
  ds_read_b64_tr_b16 a[152:153], v40 offset:32768
  ds_read_b64_tr_b16 a[154:155], v40 offset:34816
  ds_read_b64_tr_b16 a[156:157], v41 offset:32768
  ds_read_b64_tr_b16 a[158:159], v41 offset:34816
LS18_%=:
  s_cbranch_execz LS22_%=
  s_add_u32 m0, s63, 0x1000
  s_add_u32 s72, s72, 0x1c000
  buffer_load_dwordx4 v30, s[36:39], s72 offen lds
  buffer_load_dwordx4 v31, s[36:39], s72 offen offset:1024 lds
  buffer_load_dwordx4 v32, s[36:39], s72 offen offset:2048 lds
  buffer_load_dwordx4 v33, s[36:39], s72 offen offset:3072 lds
  ds_read_b128 v[70:73], v14 offset:640
  s_waitcnt lgkmcnt(7)
  v_mfma_f32_16x16x32_bf16 v[52:55], v[74:77], a[144:147], v[52:55]
  s_waitcnt lgkmcnt(5)
  v_mfma_f32_16x16x32_bf16 v[56:59], v[74:77], a[148:151], v[56:59]
  s_waitcnt lgkmcnt(3)
  v_mfma_f32_16x16x32_bf16 v[60:63], v[74:77], a[152:155], v[60:63]
  s_waitcnt lgkmcnt(1)
  v_mfma_f32_16x16x32_bf16 v[66:69], v[74:77], a[156:159], v[66:69]
  s_waitcnt vmcnt(8)
  ds_read_b64_tr_b16 a[160:161], v38 offset:12288
  ds_read_b64_tr_b16 a[162:163], v38 offset:14336
  ds_read_b64_tr_b16 a[164:165], v39 offset:12288
  ds_read_b64_tr_b16 a[166:167], v39 offset:14336
  ds_read_b64_tr_b16 a[168:169], v40 offset:12288
  ds_read_b64_tr_b16 a[170:171], v40 offset:14336
  ds_read_b64_tr_b16 a[172:173], v41 offset:12288
  ds_read_b64_tr_b16 a[174:175], v41 offset:14336
  buffer_load_dwordx2 v[20:21], v12, s[8:11], s51 offen sc1
  s_add_u32 m0, s63, 0x2000
  s_add_u32 s72, s72, 0x1c000
  buffer_load_dwordx4 v30, s[36:39], s72 offen lds
  buffer_load_dwordx4 v31, s[36:39], s72 offen offset:1024 lds
  buffer_load_dwordx4 v32, s[36:39], s72 offen offset:2048 lds
  buffer_load_dwordx4 v33, s[36:39], s72 offen offset:3072 lds
  ds_read_b128 v[74:77], v14 offset:704
  s_waitcnt lgkmcnt(7)
  v_mfma_f32_16x16x32_bf16 v[52:55], v[70:73], a[160:163], v[52:55]
  s_waitcnt lgkmcnt(5)
  v_mfma_f32_16x16x32_bf16 v[56:59], v[70:73], a[164:167], v[56:59]
  s_waitcnt lgkmcnt(3)
  v_mfma_f32_16x16x32_bf16 v[60:63], v[70:73], a[168:171], v[60:63]
  s_waitcnt lgkmcnt(1)
  v_mfma_f32_16x16x32_bf16 v[66:69], v[70:73], a[172:175], v[66:69]
  s_waitcnt vmcnt(9)
  ds_read_b64_tr_b16 a[176:177], v38
  ds_read_b64_tr_b16 a[178:179], v38 offset:2048
  ds_read_b64_tr_b16 a[180:181], v39
  ds_read_b64_tr_b16 a[182:183], v39 offset:2048
  ds_read_b64_tr_b16 a[184:185], v40
  ds_read_b64_tr_b16 a[186:187], v40 offset:2048
  ds_read_b64_tr_b16 a[188:189], v41
  ds_read_b64_tr_b16 a[190:191], v41 offset:2048
  s_add_u32 m0, s63, 0x8000
  s_add_u32 s72, s72, 0x1c000
  buffer_load_dwordx4 v30, s[36:39], s72 offen lds
  buffer_load_dwordx4 v31, s[36:39], s72 offen offset:1024 lds
  buffer_load_dwordx4 v32, s[36:39], s72 offen offset:2048 lds
  buffer_load_dwordx4 v33, s[36:39], s72 offen offset:3072 lds
  ds_read_b128 v[70:73], v14 offset:768
  s_waitcnt lgkmcnt(7)
  v_mfma_f32_16x16x32_bf16 v[52:55], v[74:77], a[176:179], v[52:55]
  s_waitcnt lgkmcnt(5)
  v_mfma_f32_16x16x32_bf16 v[56:59], v[74:77], a[180:183], v[56:59]
  s_waitcnt lgkmcnt(3)
  v_mfma_f32_16x16x32_bf16 v[60:63], v[74:77], a[184:187], v[60:63]
  s_waitcnt lgkmcnt(1)
  v_mfma_f32_16x16x32_bf16 v[66:69], v[74:77], a[188:191], v[66:69]
  s_waitcnt vmcnt(9)
  ds_read_b64_tr_b16 a[192:193], v38 offset:4096
  ds_read_b64_tr_b16 a[194:195], v38 offset:6144
  ds_read_b64_tr_b16 a[196:197], v39 offset:4096
  ds_read_b64_tr_b16 a[198:199], v39 offset:6144
  ds_read_b64_tr_b16 a[200:201], v40 offset:4096
  ds_read_b64_tr_b16 a[202:203], v40 offset:6144
  ds_read_b64_tr_b16 a[204:205], v41 offset:4096
  ds_read_b64_tr_b16 a[206:207], v41 offset:6144
  ds_read_b128 v[74:77], v14 offset:832
  s_waitcnt lgkmcnt(7)
  v_mfma_f32_16x16x32_bf16 v[52:55], v[70:73], a[192:195], v[52:55]
  s_waitcnt lgkmcnt(5)
  v_mfma_f32_16x16x32_bf16 v[56:59], v[70:73], a[196:199], v[56:59]
  s_waitcnt lgkmcnt(3)
  v_mfma_f32_16x16x32_bf16 v[60:63], v[70:73], a[200:203], v[60:63]
  s_waitcnt lgkmcnt(1)
  v_mfma_f32_16x16x32_bf16 v[66:69], v[70:73], a[204:207], v[66:69]
  s_waitcnt vmcnt(8)
  v_cmp_ne_u64 vcc, s[66:67], v[20:21]
  s_cmp_lg_u64 vcc, 0
  s_cbranch_scc1 L11_%=
L12_%=:
  s_add_u32 s51, s51, 0x8000
  s_add_u32 m0, s63, 0x4000
  s_nop 0
  buffer_load_dwordx4 v2, s[8:11], s4 offen sc1 lds
  buffer_load_dwordx4 v2, s[8:11], s4 offen offset:1024 sc1 lds
  buffer_load_dwordx4 v2, s[8:11], s4 offen offset:2048 sc1 lds
  buffer_load_dwordx4 v2, s[8:11], s4 offen offset:3072 sc1 lds
  s_add_u32 s4, s4, 0x4000
  s_lshl_b32 s53, s58, 11
  s_add_u32 m0, s53, 0x24000
  s_nop 0
  buffer_load_dwordx4 v2, s[8:11], s80 offen sc1 lds
  buffer_load_dwordx4 v2, s[8:11], s80 offen offset:1024 sc1 lds
  s_add_u32 m0, s63, 0x5000
  s_nop 0
  buffer_load_dwordx4 v2, s[8:11], s4 offen sc1 lds
  buffer_load_dwordx4 v2, s[8:11], s4 offen offset:1024 sc1 lds
  buffer_load_dwordx4 v2, s[8:11], s4 offen offset:2048 sc1 lds
  buffer_load_dwordx4 v2, s[8:11], s4 offen offset:3072 sc1 lds
  s_add_u32 s4, s4, 0x4000
  s_waitcnt vmcnt(14)
  ds_read_b64_tr_b16 a[208:209], v38 offset:8192
  ds_read_b64_tr_b16 a[210:211], v38 offset:10240
  ds_read_b64_tr_b16 a[212:213], v39 offset:8192
  ds_read_b64_tr_b16 a[214:215], v39 offset:10240
  ds_read_b64_tr_b16 a[216:217], v40 offset:8192
  ds_read_b64_tr_b16 a[218:219], v40 offset:10240
  ds_read_b64_tr_b16 a[220:221], v41 offset:8192
  ds_read_b64_tr_b16 a[222:223], v41 offset:10240
  s_waitcnt vmcnt(10)
  ds_read_b64_tr_b16 a[224:225], v38 offset:32768
  ds_read_b64_tr_b16 a[226:227], v38 offset:34816
  ds_read_b64_tr_b16 a[228:229], v39 offset:32768
  ds_read_b64_tr_b16 a[230:231], v39 offset:34816
  ds_read_b64_tr_b16 a[232:233], v40 offset:32768
  ds_read_b64_tr_b16 a[234:235], v40 offset:34816
  ds_read_b64_tr_b16 a[236:237], v41 offset:32768
  ds_read_b64_tr_b16 a[238:239], v41 offset:34816
  ds_read_b128 v[70:73], v14 offset:896
  s_waitcnt lgkmcnt(14)
  v_mfma_f32_16x16x32_bf16 v[52:55], v[74:77], a[208:211], v[52:55]
  s_waitcnt lgkmcnt(13)
  v_mfma_f32_16x16x32_bf16 v[56:59], v[74:77], a[212:215], v[56:59]
  s_waitcnt lgkmcnt(11)
  v_mfma_f32_16x16x32_bf16 v[60:63], v[74:77], a[216:219], v[60:63]
  s_waitcnt lgkmcnt(9)
  v_mfma_f32_16x16x32_bf16 v[66:69], v[74:77], a[220:223], v[66:69]
  ds_read_b128 v[74:77], v14 offset:960
  s_waitcnt lgkmcnt(1)
  v_mfma_f32_16x16x32_bf16 v[52:55], v[70:73], a[224:227], v[52:55]
  v_mfma_f32_16x16x32_bf16 v[56:59], v[70:73], a[228:231], v[56:59]
  v_mfma_f32_16x16x32_bf16 v[60:63], v[70:73], a[232:235], v[60:63]
  v_mfma_f32_16x16x32_bf16 v[66:69], v[70:73], a[236:239], v[66:69]
  s_add_u32 m0, s63, 0x6000
  s_nop 0
  buffer_load_dwordx4 v2, s[8:11], s4 offen sc1 lds
  buffer_load_dwordx4 v2, s[8:11], s4 offen offset:1024 sc1 lds
  buffer_load_dwordx4 v2, s[8:11], s4 offen offset:2048 sc1 lds
  buffer_load_dwordx4 v2, s[8:11], s4 offen offset:3072 sc1 lds
LS19_%=:
  s_cbranch_execz LS23_%=
  s_add_u32 s4, s4, 0x4000
  s_add_u32 m0, s63, 0x7000
  s_nop 0
  buffer_load_dwordx4 v2, s[8:11], s4 offen sc1 lds
  buffer_load_dwordx4 v2, s[8:11], s4 offen offset:1024 sc1 lds
  buffer_load_dwordx4 v2, s[8:11], s4 offen offset:2048 sc1 lds
  buffer_load_dwordx4 v2, s[8:11], s4 offen offset:3072 sc1 lds
  s_add_u32 s4, s4, 0x4000
  s_waitcnt vmcnt(12)
  s_barrier
  ds_read_b128 v[82:85], v49
  ds_read_b128 v[86:89], v49 offset:1024
  ds_read_b128 v[90:93], v49 offset:2048
  ds_read_b128 v[94:97], v49 offset:3072
  s_waitcnt lgkmcnt(3)
  v_mfma_f32_16x16x32_bf16 v[192:195], a[0:3], v[82:85], 0
  s_add_u32 m0, s63, 0x3000
  s_add_u32 s72, s72, 0x1c000
  v_lshlrev_b32 v6, 3, v1
  s_waitcnt lgkmcnt(2)
  v_mfma_f32_16x16x32_bf16 v[192:195], a[16:19], v[86:89], v[192:195]
  ds_read_b128 v[150:153], v49 offset:36864
  v_add_u32 v6, 0x24000, v6
  s_waitcnt lgkmcnt(2)
  v_mfma_f32_16x16x32_bf16 v[192:195], a[32:35], v[90:93], v[192:195]
  ds_read_b128 v[154:157], v49 offset:37888
  s_waitcnt lgkmcnt(2)
  v_mfma_f32_16x16x32_bf16 v[192:195], a[48:51], v[94:97], v[192:195]
  ds_read_b64 v[232:233], v6
  ds_read_b64 v[226:227], v6 offset:512
  v_mfma_f32_16x16x32_bf16 v[196:199], a[4:7], v[82:85], 0
  ds_read_b64 v[228:229], v6 offset:1024
  v_mfma_f32_16x16x32_bf16 v[196:199], a[20:23], v[86:89], v[196:199]
  ds_read_b128 v[166:169], v49 offset:38912
  v_mfma_f32_16x16x32_bf16 v[196:199], a[36:39], v[90:93], v[196:199]
  ds_read_b64 v[230:231], v6 offset:1536
  ds_read_b64 v[224:225], v6 offset:2048
  v_mfma_f32_16x16x32_bf16 v[196:199], a[52:55], v[94:97], v[196:199]
  buffer_load_dwordx4 v30, s[36:39], s72 offen lds
  v_mfma_f32_16x16x32_bf16 v[200:203], a[8:11], v[82:85], 0
  ds_read_b128 v[252:255], v49 offset:39936
  s_waitcnt lgkmcnt(5)
  v_add_f32 v232, v232, v226
  v_mfma_f32_16x16x32_bf16 v[200:203], a[24:27], v[86:89], v[200:203]
  buffer_load_dwordx4 v31, s[36:39], s72 offen offset:1024 lds
  v_mfma_f32_16x16x32_bf16 v[200:203], a[40:43], v[90:93], v[200:203]
  v_add_f32 v233, v233, v227
  ds_read_b64 v[226:227], v6 offset:2560
  v_mfma_f32_16x16x32_bf16 v[200:203], a[56:59], v[94:97], v[200:203]
  s_waitcnt lgkmcnt(5)
  v_add_f32 v232, v232, v228
  v_mfma_f32_16x16x32_bf16 v[204:207], a[12:15], v[82:85], 0
  buffer_load_dwordx4 v32, s[36:39], s72 offen offset:2048 lds
  v_mfma_f32_16x16x32_bf16 v[204:207], a[28:31], v[86:89], v[204:207]
  v_add_f32 v233, v233, v229
  ds_read_b64 v[228:229], v6 offset:3072
  v_mfma_f32_16x16x32_bf16 v[204:207], a[44:47], v[90:93], v[204:207]
  s_waitcnt lgkmcnt(4)
  v_add_f32 v232, v232, v230
  v_mfma_f32_16x16x32_bf16 v[204:207], a[60:63], v[94:97], v[204:207]
  s_waitcnt lgkmcnt(2)
  buffer_load_dwordx4 v33, s[36:39], s72 offen offset:3072 lds
  v_mfma_f32_16x16x32_bf16 v[192:195], a[64:67], v[150:153], v[192:195]
  ds_read_b128 v[82:85], v50
  v_add_f32 v233, v233, v231
  v_mfma_f32_16x16x32_bf16 v[192:195], a[80:83], v[154:157], v[192:195]
  ds_read_b64 v[230:231], v6 offset:3584
  v_mfma_f32_16x16x32_bf16 v[192:195], a[96:99], v[166:169], v[192:195]
  ds_read_b128 v[86:89], v50 offset:1024
  s_add_u32 m0, s63, 0
  s_add_u32 s73, s73, 0x8000
  v_mfma_f32_16x16x32_bf16 v[192:195], a[112:115], v[252:255], v[192:195]
  v_add_f32 v232, v232, v224
  v_add_f32 v233, v233, v225
  v_mfma_f32_16x16x32_bf16 v[196:199], a[68:71], v[150:153], v[196:199]
  ds_read_b64 v[224:225], v6 offset:4096
  v_mfma_f32_16x16x32_bf16 v[196:199], a[84:87], v[154:157], v[196:199]
  ds_read_b128 v[90:93], v50 offset:2048
  s_waitcnt lgkmcnt(6)
  v_add_f32 v232, v232, v226
  v_mfma_f32_16x16x32_bf16 v[196:199], a[100:103], v[166:169], v[196:199]
  buffer_load_dwordx4 v34, s[40:43], s73 offen lds
  v_mfma_f32_16x16x32_bf16 v[196:199], a[116:119], v[252:255], v[196:199]
  v_add_f32 v233, v233, v227
  v_mfma_f32_16x16x32_bf16 v[200:203], a[72:75], v[150:153], v[200:203]
  ds_read_b128 v[94:97], v50 offset:3072
  v_mfma_f32_16x16x32_bf16 v[200:203], a[88:91], v[154:157], v[200:203]
  ds_read_b64 v[226:227], v6 offset:4608
  s_waitcnt lgkmcnt(7)
  v_add_f32 v232, v232, v228
  v_mfma_f32_16x16x32_bf16 v[200:203], a[104:107], v[166:169], v[200:203]
  buffer_load_dwordx4 v35, s[40:43], s73 offen offset:1024 lds
  v_mfma_f32_16x16x32_bf16 v[200:203], a[120:123], v[252:255], v[200:203]
  v_add_f32 v233, v233, v229
  ds_read_b64 v[228:229], v6 offset:5120
  v_mfma_f32_16x16x32_bf16 v[204:207], a[76:79], v[150:153], v[204:207]
  buffer_load_dwordx4 v36, s[40:43], s73 offen offset:2048 lds
  v_mfma_f32_16x16x32_bf16 v[204:207], a[92:95], v[154:157], v[204:207]
  s_waitcnt lgkmcnt(6)
  v_add_f32 v232, v232, v230
  v_add_f32 v233, v233, v231
  v_mfma_f32_16x16x32_bf16 v[204:207], a[108:111], v[166:169], v[204:207]
  ds_read_b64 v[230:231], v6 offset:5632
  v_mfma_f32_16x16x32_bf16 v[204:207], a[124:127], v[252:255], v[204:207]
  s_waitcnt lgkmcnt(3)
  buffer_load_dwordx4 v37, s[40:43], s73 offen offset:3072 lds
  v_mfma_f32_16x16x32_bf16 v[192:195], a[128:131], v[82:85], v[192:195]
  ds_read_b128 v[150:153], v50 offset:36864
  v_add_f32 v232, v232, v224
  v_mfma_f32_16x16x32_bf16 v[192:195], a[144:147], v[86:89], v[192:195]
  v_add_f32 v233, v233, v225
  v_mfma_f32_16x16x32_bf16 v[192:195], a[160:163], v[90:93], v[192:195]
  ds_read_b128 v[154:157], v50 offset:37888
  v_mfma_f32_16x16x32_bf16 v[192:195], a[176:179], v[94:97], v[192:195]
  ds_read_b64 v[224:225], v6 offset:6144
  s_add_u32 m0, s63, 0x1000
  s_add_u32 s73, s73, 0x8000
  v_mfma_f32_16x16x32_bf16 v[196:199], a[132:135], v[82:85], v[196:199]
  buffer_load_dwordx4 v34, s[40:43], s73 offen lds
  v_mfma_f32_16x16x32_bf16 v[196:199], a[148:151], v[86:89], v[196:199]
  ds_read_b128 v[166:169], v50 offset:38912
  s_waitcnt lgkmcnt(6)
  v_add_f32 v232, v232, v226
  v_mfma_f32_16x16x32_bf16 v[196:199], a[164:167], v[90:93], v[196:199]
  v_add_f32 v233, v233, v227
  ds_read_b64 v[226:227], v6 offset:6656
  v_mfma_f32_16x16x32_bf16 v[196:199], a[180:183], v[94:97], v[196:199]
  s_waitcnt lgkmcnt(6)
  v_add_f32 v232, v232, v228
  v_mfma_f32_16x16x32_bf16 v[200:203], a[136:139], v[82:85], v[200:203]
  ds_read_b128 v[252:255], v50 offset:39936
  v_add_f32 v233, v233, v229
  v_mfma_f32_16x16x32_bf16 v[200:203], a[152:155], v[86:89], v[200:203]
  ds_read_b64 v[228:229], v6 offset:7168
  v_mfma_f32_16x16x32_bf16 v[200:203], a[168:171], v[90:93], v[200:203]
  buffer_load_dwordx4 v35, s[40:43], s73 offen offset:1024 lds
  v_mfma_f32_16x16x32_bf16 v[200:203], a[184:187], v[94:97], v[200:203]
  s_waitcnt lgkmcnt(7)
  v_add_f32 v232, v232, v230
  v_add_f32 v233, v233, v231
  v_mfma_f32_16x16x32_bf16 v[204:207], a[140:143], v[82:85], v[204:207]
  ds_read_b64 v[230:231], v6 offset:7680
  v_mfma_f32_16x16x32_bf16 v[204:207], a[156:159], v[86:89], v[204:207]
  buffer_load_dwordx4 v36, s[40:43], s73 offen offset:2048 lds
  v_mfma_f32_16x16x32_bf16 v[204:207], a[172:175], v[90:93], v[204:207]
  s_waitcnt lgkmcnt(5)
  v_add_f32 v232, v232, v224
  v_add_f32 v233, v233, v225
  v_mfma_f32_16x16x32_bf16 v[204:207], a[188:191], v[94:97], v[204:207]
  s_waitcnt lgkmcnt(2)
  s_waitcnt vmcnt(11)
LS20_%=:
  s_cbranch_execz LS24_%=
  s_barrier
  buffer_load_dwordx4 v37, s[40:43], s73 offen offset:3072 lds
  s_waitcnt vmcnt(8)
  ds_read_b64_tr_b16 a[240:241], v38 offset:12288
  ds_read_b64_tr_b16 a[242:243], v38 offset:14336
  ds_read_b64_tr_b16 a[244:245], v39 offset:12288
  ds_read_b64_tr_b16 a[246:247], v39 offset:14336
  ds_read_b64_tr_b16 a[248:249], v40 offset:12288
  ds_read_b64_tr_b16 a[250:251], v40 offset:14336
  ds_read_b64_tr_b16 a[252:253], v41 offset:12288
  ds_read_b64_tr_b16 a[254:255], v41 offset:14336
  v_mfma_f32_16x16x32_bf16 v[192:195], a[192:195], v[150:153], v[192:195]
  s_add_u32 m0, s63, 0x2000
  s_add_u32 s73, s73, 0x8000
  v_add_f32 v232, v232, v226
  v_mfma_f32_16x16x32_bf16 v[192:195], a[208:211], v[154:157], v[192:195]
  ds_read_b128 v[82:85], v49 offset:4096
  s_waitcnt lgkmcnt(7)
  v_mfma_f32_16x16x32_bf16 v[52:55], v[74:77], a[240:243], v[52:55]
  buffer_load_dwordx4 v34, s[40:43], s73 offen lds
  v_mfma_f32_16x16x32_bf16 v[192:195], a[224:227], v[166:169], v[192:195]
  ds_read_b128 v[86:89], v49 offset:5120
  s_waitcnt lgkmcnt(6)
  v_mfma_f32_16x16x32_bf16 v[56:59], v[74:77], a[244:247], v[56:59]
  buffer_load_dwordx4 v35, s[40:43], s73 offen offset:1024 lds
  v_mfma_f32_16x16x32_bf16 v[192:195], a[240:243], v[252:255], v[192:195]
  s_waitcnt lgkmcnt(4)
  v_mfma_f32_16x16x32_bf16 v[60:63], v[74:77], a[248:251], v[60:63]
  s_waitcnt lgkmcnt(2)
  v_mfma_f32_16x16x32_bf16 v[66:69], v[74:77], a[252:255], v[66:69]
  buffer_load_dwordx4 v36, s[40:43], s73 offen offset:2048 lds
  v_mfma_f32_16x16x32_bf16 v[196:199], a[196:199], v[150:153], v[196:199]
  v_add_f32 v233, v233, v227
  v_add_f32 v232, v232, v228
  v_mfma_f32_16x16x32_bf16 v[196:199], a[212:215], v[154:157], v[196:199]
  ds_read_b128 v[90:93], v49 offset:6144
  v_add_f32 v233, v233, v229
  v_add_f32 v232, v232, v230
  v_mfma_f32_16x16x32_bf16 v[196:199], a[228:231], v[166:169], v[196:199]
  v_mfma_f32_16x16x32_bf16 v[196:199], a[244:247], v[252:255], v[196:199]
  buffer_load_dwordx4 v37, s[40:43], s73 offen offset:3072 lds
  v_mfma_f32_16x16x32_bf16 v[200:203], a[200:203], v[150:153], v[200:203]
  ds_read_b128 v[94:97], v49 offset:7168
  v_add_f32 v233, v233, v231
  v_mul_f32 v232, 0xbb000000, v232
  v_mfma_f32_16x16x32_bf16 v[200:203], a[216:219], v[154:157], v[200:203]
  v_mfma_f32_16x16x32_bf16 v[200:203], a[232:235], v[166:169], v[200:203]
  s_add_u32 m0, s63, 0x8000
  s_add_u32 s73, s73, 0x8000
  v_mul_f32 v233, 0x3b000000, v233
  v_mfma_f32_16x16x32_bf16 v[200:203], a[248:251], v[252:255], v[200:203]
  buffer_load_dwordx4 v34, s[40:43], s73 offen lds
  v_mfma_f32_16x16x32_bf16 v[204:207], a[204:207], v[150:153], v[204:207]
  v_fma_f32 v233, -v232, v232, v233
  v_mfma_f32_16x16x32_bf16 v[204:207], a[220:223], v[154:157], v[204:207]
  v_max_f32 v233, 0, v233
  v_mfma_f32_16x16x32_bf16 v[204:207], a[236:239], v[166:169], v[204:207]
  v_add_f32 v233, 0x3727c5ac, v233
  v_mfma_f32_16x16x32_bf16 v[204:207], a[252:255], v[252:255], v[204:207]
  buffer_load_dwordx4 v35, s[40:43], s73 offen offset:1024 lds
  s_waitcnt lgkmcnt(0)
  v_mfma_f32_16x16x32_bf16 v[208:211], a[0:3], v[82:85], 0
  ds_read_b128 v[150:153], v49 offset:40960
  v_rsq_f32 v238, v233
  v_mov_b32 v242, 0
  v_mfma_f32_16x16x32_bf16 v[208:211], a[16:19], v[86:89], v[208:211]
  v_mul_f32 v233, v238, v233
  v_mfma_f32_16x16x32_bf16 v[208:211], a[32:35], v[90:93], v[208:211]
  ds_read_b128 v[154:157], v49 offset:41984
  v_mov_b32 v243, 0
  v_mfma_f32_16x16x32_bf16 v[208:211], a[48:51], v[94:97], v[208:211]
  buffer_load_dwordx4 v36, s[40:43], s73 offen offset:2048 lds
  v_mfma_f32_16x16x32_bf16 v[212:215], a[4:7], v[82:85], 0
  v_permlane16_swap_b32 v232, v233
  v_cvt_pk_bf16_f32 v235, v232, 0
  v_mfma_f32_16x16x32_bf16 v[212:215], a[20:23], v[86:89], v[212:215]
  ds_read_b128 v[166:169], v49 offset:43008
  v_lshlrev_b32 v234, 16, v235
  v_mfma_f32_16x16x32_bf16 v[212:215], a[36:39], v[90:93], v[212:215]
  v_sub_f32 v234, v232, v234
  v_mfma_f32_16x16x32_bf16 v[212:215], a[52:55], v[94:97], v[212:215]
  buffer_load_dwordx4 v37, s[40:43], s73 offen offset:3072 lds
  v_mfma_f32_16x16x32_bf16 v[216:219], a[8:11], v[82:85], 0
  ds_read_b128 v[252:255], v49 offset:44032
  v_cvt_pk_bf16_f32 v234, v232, v234
  v_cvt_pk_bf16_f32 v237, v233, 0
  v_mfma_f32_16x16x32_bf16 v[216:219], a[24:27], v[86:89], v[216:219]
  v_lshlrev_b32 v236, 16, v237
  v_mfma_f32_16x16x32_bf16 v[216:219], a[40:43], v[90:93], v[216:219]
  s_add_u32 m0, s63, 0x4000
  s_add_u32 s73, s73, 0x8000
  v_sub_f32 v236, v233, v236
  v_mfma_f32_16x16x32_bf16 v[216:219], a[56:59], v[94:97], v[216:219]
  buffer_load_dwordx4 v34, s[40:43], s73 offen lds
  v_mfma_f32_16x16x32_bf16 v[220:223], a[12:15], v[82:85], 0
  v_cvt_pk_bf16_f32 v236, v233, v236
  s_waitcnt vmcnt(13)
  ds_read_b64_tr_b16 v[78:79], v42
  v_mfma_f32_16x16x32_bf16 v[220:223], a[28:31], v[86:89], v[220:223]
  ds_read_b64_tr_b16 v[80:81], v42 offset:2048
  ds_read_b64_tr_b16 v[98:99], v43
  v_cvt_pk_bf16_f32 v5, v52, v52
  v_mfma_f32_16x16x32_bf16 v[220:223], a[44:47], v[90:93], v[220:223]
  ds_read_b64_tr_b16 v[100:101], v43 offset:2048
  ds_read_b64_tr_b16 v[102:103], v44
  v_lshlrev_b32 v53, 16, v5
  v_mfma_f32_16x16x32_bf16 v[220:223], a[60:63], v[94:97], v[220:223]
  buffer_load_dwordx4 v35, s[40:43], s73 offen offset:1024 lds
  v_sub_f32 v53, v52, v53
  s_waitcnt lgkmcnt(5)
  v_mfma_f32_16x16x32_bf16 v[208:211], a[64:67], v[150:153], v[208:211]
  ds_read_b128 v[82:85], v50 offset:4096
  v_cvt_pk_bf16_f32 v53, v53, 0
  v_mov_b32 v52, v5
  v_mfma_f32_16x16x32_bf16 v[208:211], a[80:83], v[154:157], v[208:211]
  ds_read_b64_tr_b16 v[104:105], v44 offset:2048
  ds_read_b64_tr_b16 v[106:107], v45
  v_cvt_pk_bf16_f32 v5, v56, v56
  v_mfma_f32_16x16x32_bf16 v[208:211], a[96:99], v[166:169], v[208:211]
  ds_read_b128 v[86:89], v50 offset:5120
  v_lshlrev_b32 v57, 16, v5
  v_mfma_f32_16x16x32_bf16 v[208:211], a[112:115], v[252:255], v[208:211]
  buffer_load_dwordx4 v36, s[40:43], s73 offen offset:2048 lds
  v_mfma_f32_16x16x32_bf16 v[212:215], a[68:71], v[150:153], v[212:215]
  ds_read_b64_tr_b16 v[108:109], v45 offset:2048
  v_sub_f32 v57, v56, v57
  v_cvt_pk_bf16_f32 v57, v57, 0
  v_mfma_f32_16x16x32_bf16 v[212:215], a[84:87], v[154:157], v[212:215]
  ds_read_b128 v[90:93], v50 offset:6144
  v_mov_b32 v56, v5
  v_mfma_f32_16x16x32_bf16 v[212:215], a[100:103], v[166:169], v[212:215]
  v_cvt_pk_bf16_f32 v5, v60, v60
  v_mfma_f32_16x16x32_bf16 v[212:215], a[116:119], v[252:255], v[212:215]
  buffer_load_dwordx4 v37, s[40:43], s73 offen offset:3072 lds
  v_mfma_f32_16x16x32_bf16 v[216:219], a[72:75], v[150:153], v[216:219]
  ds_read_b128 v[94:97], v50 offset:7168
  v_lshlrev_b32 v61, 16, v5
  v_sub_f32 v61, v60, v61
  v_mfma_f32_16x16x32_bf16 v[216:219], a[88:91], v[154:157], v[216:219]
  v_cvt_pk_bf16_f32 v61, v61, 0
  s_add_u32 m0, s63, 0x3000
  v_mfma_f32_16x16x32_bf16 v[216:219], a[104:107], v[166:169], v[216:219]
  s_add_u32 s73, s73, 0x8000
  v_mov_b32 v60, v5
  v_mfma_f32_16x16x32_bf16 v[216:219], a[120:123], v[252:255], v[216:219]
LS21_%=:
  s_cbranch_execz LS25_%=
  buffer_load_dwordx4 v34, s[40:43], s73 offen lds
  v_mfma_f32_16x16x32_bf16 v[220:223], a[76:79], v[150:153], v[220:223]
  v_cvt_pk_bf16_f32 v5, v66, v66
  v_lshlrev_b32 v67, 16, v5
  v_mfma_f32_16x16x32_bf16 v[220:223], a[92:95], v[154:157], v[220:223]
  v_sub_f32 v67, v66, v67
  v_mfma_f32_16x16x32_bf16 v[220:223], a[108:111], v[166:169], v[220:223]
  v_cvt_pk_bf16_f32 v67, v67, 0
  v_mfma_f32_16x16x32_bf16 v[220:223], a[124:127], v[252:255], v[220:223]
  buffer_load_dwordx4 v35, s[40:43], s73 offen offset:1024 lds
  v_mov_b32 v66, v5
  s_waitcnt lgkmcnt(0)
  v_mfma_f32_16x16x32_bf16 v[208:211], a[128:131], v[82:85], v[208:211]
  ds_read_b128 v[150:153], v50 offset:40960
  v_cndmask_b32 v240, v7, v234, s[6:7]
  v_cndmask_b32 v241, v7, v235, s[6:7]
  v_mfma_f32_16x16x32_bf16 v[208:211], a[144:147], v[86:89], v[208:211]
  s_waitcnt vmcnt(14)
  ds_read_b64_tr_b16 v[110:111], v42 offset:4096
  ds_read_b64_tr_b16 v[112:113], v42 offset:6144
  v_mfma_f32_16x16x32_bf16 v[208:211], a[160:163], v[90:93], v[208:211]
  ds_read_b128 v[154:157], v50 offset:41984
  v_mfma_f32_16x16x32_bf16 v[192:195], v[52:55], v[240:243], v[192:195]
  buffer_load_dwordx4 v36, s[40:43], s73 offen offset:2048 lds
  v_mfma_f32_16x16x32_bf16 v[208:211], a[176:179], v[94:97], v[208:211]
  ds_read_b64_tr_b16 v[114:115], v43 offset:4096
  ds_read_b64_tr_b16 v[116:117], v43 offset:6144
  v_mfma_f32_16x16x32_bf16 v[196:199], v[56:59], v[240:243], v[196:199]
  ds_read_b64_tr_b16 v[118:119], v44 offset:4096
  ds_read_b64_tr_b16 v[120:121], v44 offset:6144
  v_mfma_f32_16x16x32_bf16 v[212:215], a[132:135], v[82:85], v[212:215]
  ds_read_b64_tr_b16 v[122:123], v45 offset:4096
  v_mfma_f32_16x16x32_bf16 v[200:203], v[60:63], v[240:243], v[200:203]
  buffer_load_dwordx4 v37, s[40:43], s73 offen offset:3072 lds
  v_mfma_f32_16x16x32_bf16 v[212:215], a[148:151], v[86:89], v[212:215]
  ds_read_b128 v[166:169], v50 offset:43008
  v_mfma_f32_16x16x32_bf16 v[204:207], v[66:69], v[240:243], v[204:207]
  ds_read_b64_tr_b16 v[124:125], v45 offset:6144
  v_mfma_f32_16x16x32_bf16 v[212:215], a[164:167], v[90:93], v[212:215]
  s_add_u32 m0, s63, 0
  s_add_u32 s73, s73, 0x8000
  v_cndmask_b32 v240, v7, v236, s[6:7]
  v_mfma_f32_16x16x32_bf16 v[212:215], a[180:183], v[94:97], v[212:215]
  buffer_load_dwordx4 v34, s[40:43], s73 offen lds
  v_mfma_f32_16x16x32_bf16 v[216:219], a[136:139], v[82:85], v[216:219]
  ds_read_b128 v[252:255], v50 offset:44032
  v_cndmask_b32 v241, v7, v237, s[6:7]
  v_mfma_f32_16x16x32_bf16 v[216:219], a[152:155], v[86:89], v[216:219]
  s_nop 0
  v_mfma_f32_16x16x32_bf16 v[208:211], v[52:55], v[240:243], v[208:211]
  v_mfma_f32_16x16x32_bf16 v[216:219], a[168:171], v[90:93], v[216:219]
  buffer_load_dwordx4 v35, s[40:43], s73 offen offset:1024 lds
  v_mfma_f32_16x16x32_bf16 v[212:215], v[56:59], v[240:243], v[212:215]
  s_waitcnt vmcnt(14)
  ds_read_b64_tr_b16 v[126:127], v42 offset:8192
  ds_read_b64_tr_b16 v[128:129], v42 offset:10240
  v_mfma_f32_16x16x32_bf16 v[216:219], a[184:187], v[94:97], v[216:219]
  ds_read_b64_tr_b16 v[130:131], v43 offset:8192
  v_mfma_f32_16x16x32_bf16 v[216:219], v[60:63], v[240:243], v[216:219]
  ds_read_b64_tr_b16 v[132:133], v43 offset:10240
  ds_read_b64_tr_b16 v[134:135], v44 offset:8192
  v_mfma_f32_16x16x32_bf16 v[220:223], a[140:143], v[82:85], v[220:223]
  buffer_load_dwordx4 v36, s[40:43], s73 offen offset:2048 lds
  v_mfma_f32_16x16x32_bf16 v[220:223], v[66:69], v[240:243], v[220:223]
  ds_read_b64_tr_b16 v[136:137], v44 offset:10240
  ds_read_b64_tr_b16 v[138:139], v45 offset:8192
  v_mfma_f32_16x16x32_bf16 v[220:223], a[156:159], v[86:89], v[220:223]
  s_add_u32 s5, s18, 0
  ds_read_b64_tr_b16 v[140:141], v45 offset:10240
  v_mfma_f32_16x16x32_bf16 v[220:223], a[172:175], v[90:93], v[220:223]
  v_cvt_pk_bf16_f32 v244, v192, v193
  v_mfma_f32_16x16x32_bf16 v[220:223], a[188:191], v[94:97], v[220:223]
  buffer_load_dwordx4 v37, s[40:43], s73 offen offset:3072 lds
  v_cvt_pk_bf16_f32 v245, v194, v195
  s_waitcnt lgkmcnt(8)
  v_mfma_f32_16x16x32_bf16 v[208:211], a[192:195], v[150:153], v[208:211]
  ds_read_b128 v[82:85], v49 offset:8192
  v_cvt_pk_bf16_f32 v246, v196, v197
  v_mfma_f32_16x16x32_bf16 v[208:211], a[208:211], v[154:157], v[208:211]
  v_cvt_pk_bf16_f32 v247, v198, v199
  s_add_u32 m0, s63, 0x1000
  v_mfma_f32_16x16x32_bf16 v[208:211], a[224:227], v[166:169], v[208:211]
  ds_read_b128 v[86:89], v49 offset:9216
  s_add_u32 s73, s73, 0x8000
  v_pk_max_i16 v244, v244, 0
  v_mfma_f32_16x16x32_bf16 v[208:211], a[240:243], v[252:255], v[208:211]
  buffer_load_dwordx4 v34, s[40:43], s73 offen lds
  v_mfma_f32_16x16x32_bf16 v[212:215], a[196:199], v[150:153], v[212:215]
  v_pk_max_i16 v245, v245, 0
  v_pk_max_i16 v246, v246, 0
  v_mfma_f32_16x16x32_bf16 v[212:215], a[212:215], v[154:157], v[212:215]
  ds_read_b128 v[90:93], v49 offset:10240
  v_pk_max_i16 v247, v247, 0
  v_mfma_f32_16x16x32_bf16 v[212:215], a[228:231], v[166:169], v[212:215]
  buffer_store_dwordx4 v[244:247], v13, s[8:11], s5 offen sc1
  v_mfma_f32_16x16x32_bf16 v[212:215], a[244:247], v[252:255], v[212:215]
  buffer_load_dwordx4 v35, s[40:43], s73 offen offset:1024 lds
  v_mfma_f32_16x16x32_bf16 v[216:219], a[200:203], v[150:153], v[216:219]
  ds_read_b128 v[94:97], v49 offset:11264
  v_cvt_pk_bf16_f32 v248, v200, v201
  v_cvt_pk_bf16_f32 v249, v202, v203
  v_mfma_f32_16x16x32_bf16 v[216:219], a[216:219], v[154:157], v[216:219]
  s_waitcnt vmcnt(15)
  ds_read_b64_tr_b16 v[142:143], v42 offset:32768
  ds_read_b64_tr_b16 v[144:145], v42 offset:34816
  v_mfma_f32_16x16x32_bf16 v[216:219], a[232:235], v[166:169], v[216:219]
  ds_read_b64_tr_b16 v[146:147], v43 offset:32768
  v_cvt_pk_bf16_f32 v250, v204, v205
  ds_read_b64_tr_b16 v[148:149], v43 offset:34816
  v_mfma_f32_16x16x32_bf16 v[216:219], a[248:251], v[252:255], v[216:219]
  buffer_load_dwordx4 v36, s[40:43], s73 offen offset:2048 lds
  v_mfma_f32_16x16x32_bf16 v[220:223], a[204:207], v[150:153], v[220:223]
  ds_read_b64_tr_b16 v[158:159], v44 offset:32768
  ds_read_b64_tr_b16 v[160:161], v44 offset:34816
  v_cvt_pk_bf16_f32 v251, v206, v207
  v_mfma_f32_16x16x32_bf16 v[220:223], a[220:223], v[154:157], v[220:223]
  ds_read_b64_tr_b16 v[162:163], v45 offset:32768
  ds_read_b64_tr_b16 v[164:165], v45 offset:34816
  v_pk_max_i16 v248, v248, 0
  v_mfma_f32_16x16x32_bf16 v[220:223], a[236:239], v[166:169], v[220:223]
  v_pk_max_i16 v249, v249, 0
  v_pk_max_i16 v250, v250, 0
  v_mfma_f32_16x16x32_bf16 v[220:223], a[252:255], v[252:255], v[220:223]
  buffer_load_dwordx4 v37, s[40:43], s73 offen offset:3072 lds
  v_pk_max_i16 v251, v251, 0
  buffer_store_dwordx4 v[248:251], v13, s[8:11], s5 offen offset:1024 sc1
  s_waitcnt lgkmcnt(8)
  v_mfma_f32_16x16x32_bf16 v[192:195], a[0:3], v[82:85], 0
  ds_read_b128 v[150:153], v49 offset:45056
  v_mfma_f32_16x16x32_bf16 v[192:195], a[16:19], v[86:89], v[192:195]
  s_add_u32 m0, s63, 0x2000
  s_add_u32 s73, s73, 0x8000
  v_mfma_f32_16x16x32_bf16 v[192:195], a[32:35], v[90:93], v[192:195]
LS22_%=:
  s_cbranch_execz LS26_%=
  ds_read_b128 v[154:157], v49 offset:46080
  s_add_u32 s5, s18, 0xe000
  v_cvt_pk_bf16_f32 v244, v208, v209
  v_mfma_f32_16x16x32_bf16 v[192:195], a[48:51], v[94:97], v[192:195]
  buffer_load_dwordx4 v34, s[40:43], s73 offen lds
  v_mfma_f32_16x16x32_bf16 v[196:199], a[4:7], v[82:85], 0
  v_cvt_pk_bf16_f32 v245, v210, v211
  v_cvt_pk_bf16_f32 v246, v212, v213
  v_mfma_f32_16x16x32_bf16 v[196:199], a[20:23], v[86:89], v[196:199]
  ds_read_b128 v[166:169], v49 offset:47104
  v_cvt_pk_bf16_f32 v247, v214, v215
  v_pk_max_i16 v244, v244, 0
  v_pk_max_i16 v245, v245, 0
  v_mfma_f32_16x16x32_bf16 v[196:199], a[36:39], v[90:93], v[196:199]
  buffer_load_dwordx4 v35, s[40:43], s73 offen offset:1024 lds
  v_pk_max_i16 v246, v246, 0
  v_pk_max_i16 v247, v247, 0
  buffer_store_dwordx4 v[244:247], v13, s[8:11], s5 offen sc1
  v_mfma_f32_16x16x32_bf16 v[196:199], a[52:55], v[94:97], v[196:199]
  v_cvt_pk_bf16_f32 v248, v216, v217
  v_cvt_pk_bf16_f32 v249, v218, v219
  v_cvt_pk_bf16_f32 v250, v220, v221
  v_mfma_f32_16x16x32_bf16 v[200:203], a[8:11], v[82:85], 0
  ds_read_b128 v[252:255], v49 offset:48128
  v_cvt_pk_bf16_f32 v251, v222, v223
  v_pk_max_i16 v248, v248, 0
  v_pk_max_i16 v249, v249, 0
  v_mfma_f32_16x16x32_bf16 v[200:203], a[24:27], v[86:89], v[200:203]
  buffer_load_dwordx4 v36, s[40:43], s73 offen offset:2048 lds
  v_pk_max_i16 v250, v250, 0
  v_pk_max_i16 v251, v251, 0
  buffer_store_dwordx4 v[248:251], v13, s[8:11], s5 offen offset:1024 sc1
  v_mfma_f32_16x16x32_bf16 v[200:203], a[40:43], v[90:93], v[200:203]
  v_mfma_f32_16x16x32_bf16 v[200:203], a[56:59], v[94:97], v[200:203]
  buffer_load_dwordx4 v37, s[40:43], s73 offen offset:3072 lds
  v_mfma_f32_16x16x32_bf16 v[204:207], a[12:15], v[82:85], 0
  v_mfma_f32_16x16x32_bf16 v[204:207], a[28:31], v[86:89], v[204:207]
  v_mfma_f32_16x16x32_bf16 v[204:207], a[44:47], v[90:93], v[204:207]
  s_add_u32 m0, s63, 0x8000
  s_add_u32 s73, s73, 0x8000
  v_mfma_f32_16x16x32_bf16 v[204:207], a[60:63], v[94:97], v[204:207]
  s_waitcnt lgkmcnt(4)
  buffer_load_dwordx4 v34, s[40:43], s73 offen lds
  s_waitcnt lgkmcnt(0)
  v_mfma_f32_16x16x32_bf16 v[192:195], a[64:67], v[150:153], v[192:195]
  ds_read_b128 v[82:85], v50 offset:8192
  v_cndmask_b32 v240, v234, v7, s[6:7]
  v_mfma_f32_16x16x32_bf16 v[192:195], a[80:83], v[154:157], v[192:195]
  v_mfma_f32_16x16x32_bf16 v[192:195], a[96:99], v[166:169], v[192:195]
  ds_read_b128 v[86:89], v50 offset:9216
  v_mfma_f32_16x16x32_bf16 v[192:195], a[112:115], v[252:255], v[192:195]
  buffer_load_dwordx4 v35, s[40:43], s73 offen offset:1024 lds
  v_mfma_f32_16x16x32_bf16 v[196:199], a[68:71], v[150:153], v[196:199]
  v_cndmask_b32 v241, v235, v7, s[6:7]
  v_mfma_f32_16x16x32_bf16 v[196:199], a[84:87], v[154:157], v[196:199]
  ds_read_b128 v[90:93], v50 offset:10240
  v_mfma_f32_16x16x32_bf16 v[196:199], a[100:103], v[166:169], v[196:199]
  v_mfma_f32_16x16x32_bf16 v[196:199], a[116:119], v[252:255], v[196:199]
  buffer_load_dwordx4 v36, s[40:43], s73 offen offset:2048 lds
  v_mfma_f32_16x16x32_bf16 v[200:203], a[72:75], v[150:153], v[200:203]
  ds_read_b128 v[94:97], v50 offset:11264
  v_mfma_f32_16x16x32_bf16 v[200:203], a[88:91], v[154:157], v[200:203]
  v_mfma_f32_16x16x32_bf16 v[192:195], v[52:55], v[240:243], v[192:195]
  v_mfma_f32_16x16x32_bf16 v[200:203], a[104:107], v[166:169], v[200:203]
  buffer_load_dwordx4 v37, s[40:43], s73 offen offset:3072 lds
  v_mfma_f32_16x16x32_bf16 v[200:203], a[120:123], v[252:255], v[200:203]
  v_mfma_f32_16x16x32_bf16 v[204:207], a[76:79], v[150:153], v[204:207]
  v_mfma_f32_16x16x32_bf16 v[196:199], v[56:59], v[240:243], v[196:199]
  v_mfma_f32_16x16x32_bf16 v[204:207], a[92:95], v[154:157], v[204:207]
  v_mfma_f32_16x16x32_bf16 v[204:207], a[108:111], v[166:169], v[204:207]
  v_mfma_f32_16x16x32_bf16 v[204:207], a[124:127], v[252:255], v[204:207]
  s_waitcnt lgkmcnt(0)
  v_mfma_f32_16x16x32_bf16 v[192:195], a[128:131], v[82:85], v[192:195]
  ds_read_b128 v[150:153], v50 offset:45056
  v_mfma_f32_16x16x32_bf16 v[200:203], v[60:63], v[240:243], v[200:203]
  v_mfma_f32_16x16x32_bf16 v[192:195], a[144:147], v[86:89], v[192:195]
  v_mfma_f32_16x16x32_bf16 v[192:195], a[160:163], v[90:93], v[192:195]
  ds_read_b128 v[154:157], v50 offset:46080
  v_mfma_f32_16x16x32_bf16 v[192:195], a[176:179], v[94:97], v[192:195]
  v_mfma_f32_16x16x32_bf16 v[204:207], v[66:69], v[240:243], v[204:207]
  v_mfma_f32_16x16x32_bf16 v[196:199], a[132:135], v[82:85], v[196:199]
  v_mfma_f32_16x16x32_bf16 v[196:199], a[148:151], v[86:89], v[196:199]
  ds_read_b128 v[166:169], v50 offset:47104
  v_mfma_f32_16x16x32_bf16 v[196:199], a[164:167], v[90:93], v[196:199]
  s_lshl_b32 s52, s76, 6
  v_mfma_f32_16x16x32_bf16 v[196:199], a[180:183], v[94:97], v[196:199]
  v_mfma_f32_16x16x32_bf16 v[200:203], a[136:139], v[82:85], v[200:203]
  ds_read_b128 v[252:255], v50 offset:48128
  v_mfma_f32_16x16x32_bf16 v[200:203], a[152:155], v[86:89], v[200:203]
  v_and_b32 v3, 15, v1
  v_mfma_f32_16x16x32_bf16 v[200:203], a[168:171], v[90:93], v[200:203]
  v_mfma_f32_16x16x32_bf16 v[200:203], a[184:187], v[94:97], v[200:203]
  v_mfma_f32_16x16x32_bf16 v[204:207], a[140:143], v[82:85], v[204:207]
  v_lshl_add_u32 v3, v3, 2, s52
  v_mfma_f32_16x16x32_bf16 v[204:207], a[156:159], v[86:89], v[204:207]
  v_mfma_f32_16x16x32_bf16 v[204:207], a[172:175], v[90:93], v[204:207]
  v_mfma_f32_16x16x32_bf16 v[204:207], a[188:191], v[94:97], v[204:207]
  s_waitcnt lgkmcnt(0)
  v_mfma_f32_16x16x32_bf16 v[192:195], a[192:195], v[150:153], v[192:195]
  ds_read_b128 v[82:85], v49 offset:12288
  v_add_u32 v4, 0x80, v3
  v_mfma_f32_16x16x32_bf16 v[192:195], a[208:211], v[154:157], v[192:195]
  v_mfma_f32_16x16x32_bf16 v[192:195], a[224:227], v[166:169], v[192:195]
  ds_read_b128 v[86:89], v49 offset:13312
  v_mfma_f32_16x16x32_bf16 v[192:195], a[240:243], v[252:255], v[192:195]
  v_mfma_f32_16x16x32_bf16 v[196:199], a[196:199], v[150:153], v[196:199]
  v_mfma_f32_16x16x32_bf16 v[196:199], a[212:215], v[154:157], v[196:199]
  ds_read_b128 v[90:93], v49 offset:14336
  ds_bpermute_b32 v16, v3, v238
  v_mfma_f32_16x16x32_bf16 v[196:199], a[228:231], v[166:169], v[196:199]
  v_mfma_f32_16x16x32_bf16 v[196:199], a[244:247], v[252:255], v[196:199]
  v_mfma_f32_16x16x32_bf16 v[200:203], a[200:203], v[150:153], v[200:203]
  ds_read_b128 v[94:97], v49 offset:15360
  v_mfma_f32_16x16x32_bf16 v[200:203], a[216:219], v[154:157], v[200:203]
  v_mfma_f32_16x16x32_bf16 v[200:203], a[232:235], v[166:169], v[200:203]
  ds_bpermute_b32 v17, v4, v238
  v_mfma_f32_16x16x32_bf16 v[200:203], a[248:251], v[252:255], v[200:203]
  v_mfma_f32_16x16x32_bf16 v[204:207], a[204:207], v[150:153], v[204:207]
  v_mfma_f32_16x16x32_bf16 v[204:207], a[220:223], v[154:157], v[204:207]
  v_mfma_f32_16x16x32_bf16 v[204:207], a[236:239], v[166:169], v[204:207]
  v_mfma_f32_16x16x32_bf16 v[204:207], a[252:255], v[252:255], v[204:207]
  s_waitcnt lgkmcnt(1)
  v_mfma_f32_16x16x32_bf16 v[208:211], a[0:3], v[82:85], 0
  ds_read_b128 v[150:153], v49 offset:49152
  v_mfma_f32_16x16x32_bf16 v[208:211], a[16:19], v[86:89], v[208:211]
  s_waitcnt vmcnt(5)
  s_barrier
  buffer_store_dwordx2 v[18:19], v9, s[8:11], s50 offen sc1
  s_add_u32 s50, s50, 0x8000
  v_mfma_f32_16x16x32_bf16 v[208:211], a[32:35], v[90:93], v[208:211]
  ds_read_b128 v[154:157], v49 offset:50176
  s_add_u32 s5, s19, 0
  s_add_u32 m0, s63, 0x5000
LS23_%=:
  s_cbranch_execz LS27_%=
  v_mfma_f32_16x16x32_bf16 v[208:211], a[48:51], v[94:97], v[208:211]
  s_add_u32 s73, s73, 0x8000
  v_cvt_pk_bf16_f32 v244, v192, v193
  v_cvt_pk_bf16_f32 v245, v194, v195
  v_mfma_f32_16x16x32_bf16 v[212:215], a[4:7], v[82:85], 0
  buffer_load_dwordx4 v34, s[40:43], s73 offen lds
  v_mfma_f32_16x16x32_bf16 v[212:215], a[20:23], v[86:89], v[212:215]
  ds_read_b128 v[166:169], v49 offset:51200
  v_cvt_pk_bf16_f32 v246, v196, v197
  v_cvt_pk_bf16_f32 v247, v198, v199
  v_mfma_f32_16x16x32_bf16 v[212:215], a[36:39], v[90:93], v[212:215]
  buffer_load_dwordx4 v35, s[40:43], s73 offen offset:1024 lds
  v_mfma_f32_16x16x32_bf16 v[212:215], a[52:55], v[94:97], v[212:215]
  v_pk_max_i16 v244, v244, 0
  v_pk_max_i16 v245, v245, 0
  v_mfma_f32_16x16x32_bf16 v[216:219], a[8:11], v[82:85], 0
  ds_read_b128 v[252:255], v49 offset:52224
  v_pk_max_i16 v246, v246, 0
  v_pk_max_i16 v247, v247, 0
  v_mfma_f32_16x16x32_bf16 v[216:219], a[24:27], v[86:89], v[216:219]
  buffer_store_dwordx4 v[244:247], v13, s[8:11], s5 offen sc1
  v_mfma_f32_16x16x32_bf16 v[216:219], a[40:43], v[90:93], v[216:219]
  buffer_load_dwordx4 v36, s[40:43], s73 offen offset:2048 lds
  v_mfma_f32_16x16x32_bf16 v[216:219], a[56:59], v[94:97], v[216:219]
  v_cvt_pk_bf16_f32 v248, v200, v201
  v_cvt_pk_bf16_f32 v249, v202, v203
  v_mfma_f32_16x16x32_bf16 v[220:223], a[12:15], v[82:85], 0
  v_cvt_pk_bf16_f32 v250, v204, v205
  v_cvt_pk_bf16_f32 v251, v206, v207
  v_mfma_f32_16x16x32_bf16 v[220:223], a[28:31], v[86:89], v[220:223]
  buffer_load_dwordx4 v37, s[40:43], s73 offen offset:3072 lds
  v_mfma_f32_16x16x32_bf16 v[220:223], a[44:47], v[90:93], v[220:223]
  v_pk_max_i16 v248, v248, 0
  v_pk_max_i16 v249, v249, 0
  v_mfma_f32_16x16x32_bf16 v[220:223], a[60:63], v[94:97], v[220:223]
  v_pk_max_i16 v250, v250, 0
  s_add_u32 m0, s63, 0x6000
  v_pk_max_i16 v251, v251, 0
  buffer_store_dwordx4 v[248:251], v13, s[8:11], s5 offen offset:1024 sc1
  s_waitcnt lgkmcnt(0)
  v_mfma_f32_16x16x32_bf16 v[208:211], a[64:67], v[150:153], v[208:211]
  ds_read_b128 v[82:85], v50 offset:12288
  s_add_u32 s73, s73, 0x8000
  v_cndmask_b32 v240, v236, v7, s[6:7]
  v_mfma_f32_16x16x32_bf16 v[208:211], a[80:83], v[154:157], v[208:211]
  buffer_load_dwordx4 v34, s[40:43], s73 offen lds
  v_mfma_f32_16x16x32_bf16 v[208:211], a[96:99], v[166:169], v[208:211]
  ds_read_b128 v[86:89], v50 offset:13312
  v_cndmask_b32 v241, v237, v7, s[6:7]
  v_mfma_f32_16x16x32_bf16 v[208:211], a[112:115], v[252:255], v[208:211]
  ds_read_b64_tr_b16 a[0:1], v42 offset:16384
  v_mfma_f32_16x16x32_bf16 v[212:215], a[68:71], v[150:153], v[212:215]
  buffer_load_dwordx4 v35, s[40:43], s73 offen offset:1024 lds
  v_mfma_f32_16x16x32_bf16 v[212:215], a[84:87], v[154:157], v[212:215]
  ds_read_b128 v[90:93], v50 offset:14336
  v_mfma_f32_16x16x32_bf16 v[212:215], a[100:103], v[166:169], v[212:215]
  ds_read_b64_tr_b16 a[2:3], v42 offset:18432
  ds_read_b64_tr_b16 a[4:5], v43 offset:16384
  v_mfma_f32_16x16x32_bf16 v[208:211], v[52:55], v[240:243], v[208:211]
  ds_read_b64_tr_b16 a[6:7], v43 offset:18432
  ds_read_b64_tr_b16 a[8:9], v44 offset:16384
  v_mfma_f32_16x16x32_bf16 v[212:215], a[116:119], v[252:255], v[212:215]
  buffer_load_dwordx4 v36, s[40:43], s73 offen offset:2048 lds
  v_mfma_f32_16x16x32_bf16 v[216:219], a[72:75], v[150:153], v[216:219]
  ds_read_b128 v[94:97], v50 offset:15360
  v_mfma_f32_16x16x32_bf16 v[216:219], a[88:91], v[154:157], v[216:219]
  ds_read_b64_tr_b16 a[10:11], v44 offset:18432
  ds_read_b64_tr_b16 a[12:13], v45 offset:16384
  v_mfma_f32_16x16x32_bf16 v[212:215], v[56:59], v[240:243], v[212:215]
  ds_read_b64_tr_b16 a[14:15], v45 offset:18432
  ds_read_b64_tr_b16 a[16:17], v42 offset:12288
  v_mfma_f32_16x16x32_bf16 v[216:219], a[104:107], v[166:169], v[216:219]
  buffer_load_dwordx4 v37, s[40:43], s73 offen offset:3072 lds
  v_mfma_f32_16x16x32_bf16 v[216:219], a[120:123], v[252:255], v[216:219]
  ds_read_b64_tr_b16 a[18:19], v42 offset:14336
  ds_read_b64_tr_b16 a[20:21], v43 offset:12288
  v_mfma_f32_16x16x32_bf16 v[216:219], v[60:63], v[240:243], v[216:219]
  ds_read_b64_tr_b16 a[22:23], v43 offset:14336
  ds_read_b64_tr_b16 a[24:25], v44 offset:12288
  v_mfma_f32_16x16x32_bf16 v[220:223], a[76:79], v[150:153], v[220:223]
  ds_read_b64_tr_b16 a[26:27], v44 offset:14336
  ds_read_b64_tr_b16 a[28:29], v45 offset:12288
  v_mfma_f32_16x16x32_bf16 v[220:223], a[92:95], v[154:157], v[220:223]
  ds_read_b64_tr_b16 a[30:31], v45 offset:14336
  s_add_u32 m0, s63, 0x4000
  s_add_u32 s73, s73, 0x8000
  v_mfma_f32_16x16x32_bf16 v[220:223], v[66:69], v[240:243], v[220:223]
  s_waitcnt lgkmcnt(8)
  buffer_load_dwordx4 v34, s[40:43], s73 offen lds
  v_mfma_f32_16x16x32_bf16 v[220:223], a[108:111], v[166:169], v[220:223]
  ds_read_b64_tr_b16 a[32:33], v42
  ds_read_b64_tr_b16 a[34:35], v42 offset:2048
  v_mfma_f32_16x16x32_bf16 v[220:223], a[124:127], v[252:255], v[220:223]
  ds_read_b64_tr_b16 a[36:37], v43
  ds_read_b64_tr_b16 a[38:39], v43 offset:2048
  v_mfma_f32_16x16x32_bf16 v[208:211], a[128:131], v[82:85], v[208:211]
  ds_read_b128 v[150:153], v50 offset:49152
  v_mfma_f32_16x16x32_bf16 v[208:211], a[144:147], v[86:89], v[208:211]
  buffer_load_dwordx4 v35, s[40:43], s73 offen offset:1024 lds
  v_mfma_f32_16x16x32_bf16 v[208:211], a[160:163], v[90:93], v[208:211]
  ds_read_b128 v[154:157], v50 offset:50176
  v_mfma_f32_16x16x32_bf16 v[208:211], a[176:179], v[94:97], v[208:211]
  ds_read_b64_tr_b16 a[40:41], v44
  ds_read_b64_tr_b16 a[42:43], v44 offset:2048
  v_mfma_f32_16x16x32_bf16 v[212:215], a[132:135], v[82:85], v[212:215]
  buffer_load_dwordx4 v36, s[40:43], s73 offen offset:2048 lds
  v_mfma_f32_16x16x32_bf16 v[212:215], a[148:151], v[86:89], v[212:215]
  ds_read_b128 v[166:169], v50 offset:51200
  v_mfma_f32_16x16x32_bf16 v[212:215], a[164:167], v[90:93], v[212:215]
  ds_read_b64_tr_b16 a[44:45], v45
  ds_read_b64_tr_b16 a[46:47], v45 offset:2048
  v_mfma_f32_16x16x32_bf16 v[212:215], a[180:183], v[94:97], v[212:215]
  ds_read_b64_tr_b16 a[48:49], v42 offset:4096
  ds_read_b64_tr_b16 a[50:51], v42 offset:6144
  v_mfma_f32_16x16x32_bf16 v[216:219], a[136:139], v[82:85], v[216:219]
  ds_read_b128 v[252:255], v50 offset:52224
  v_mfma_f32_16x16x32_bf16 v[216:219], a[152:155], v[86:89], v[216:219]
  buffer_load_dwordx4 v37, s[40:43], s73 offen offset:3072 lds
  v_mfma_f32_16x16x32_bf16 v[216:219], a[168:171], v[90:93], v[216:219]
  ds_read_b64_tr_b16 a[52:53], v43 offset:4096
  ds_read_b64_tr_b16 a[54:55], v43 offset:6144
  v_mfma_f32_16x16x32_bf16 v[216:219], a[184:187], v[94:97], v[216:219]
  ds_read_b64_tr_b16 a[56:57], v44 offset:4096
  ds_read_b64_tr_b16 a[58:59], v44 offset:6144
  v_mfma_f32_16x16x32_bf16 v[220:223], a[140:143], v[82:85], v[220:223]
  ds_read_b64_tr_b16 a[60:61], v45 offset:4096
  ds_read_b64_tr_b16 a[62:63], v45 offset:6144
  s_add_u32 m0, s63, 0x3000
LS24_%=:
  s_cbranch_execz L4_%=
  v_mfma_f32_16x16x32_bf16 v[220:223], a[156:159], v[86:89], v[220:223]
  s_add_u32 s73, s73, 0x8000
  v_mfma_f32_16x16x32_bf16 v[220:223], a[172:175], v[90:93], v[220:223]
  s_waitcnt lgkmcnt(14)
  buffer_load_dwordx4 v34, s[40:43], s73 offen lds
  v_mfma_f32_16x16x32_bf16 v[220:223], a[188:191], v[94:97], v[220:223]
  s_waitcnt vmcnt(20)
  ds_read_b64_tr_b16 a[64:65], v42 offset:8192
  ds_read_b64_tr_b16 a[66:67], v42 offset:10240
  s_waitcnt lgkmcnt(8)
  v_mfma_f32_16x16x32_bf16 v[208:211], a[192:195], v[150:153], v[208:211]
  buffer_load_dwordx2 v[20:21], v12, s[8:11], s51 offen sc1
  v_mfma_f32_16x16x32_bf16 v[208:211], a[208:211], v[154:157], v[208:211]
  buffer_load_dwordx4 v35, s[40:43], s73 offen offset:1024 lds
  v_mfma_f32_16x16x32_bf16 v[208:211], a[224:227], v[166:169], v[208:211]
  ds_read_b64_tr_b16 a[68:69], v43 offset:8192
  ds_read_b64_tr_b16 a[70:71], v43 offset:10240
  v_mfma_f32_16x16x32_bf16 v[208:211], a[240:243], v[252:255], v[208:211]
  ds_read_b64_tr_b16 a[72:73], v44 offset:8192
  ds_read_b64_tr_b16 a[74:75], v44 offset:10240
  v_mfma_f32_16x16x32_bf16 v[212:215], a[196:199], v[150:153], v[212:215]
  buffer_load_dwordx4 v36, s[40:43], s73 offen offset:2048 lds
  v_mfma_f32_16x16x32_bf16 v[212:215], a[212:215], v[154:157], v[212:215]
  ds_read_b64_tr_b16 a[76:77], v45 offset:8192
  ds_read_b64_tr_b16 a[78:79], v45 offset:10240
  v_mfma_f32_16x16x32_bf16 v[212:215], a[228:231], v[166:169], v[212:215]
  s_waitcnt vmcnt(19)
  ds_read_b64_tr_b16 a[80:81], v42 offset:32768
  ds_read_b64_tr_b16 a[82:83], v42 offset:34816
  v_mfma_f32_16x16x32_bf16 v[212:215], a[244:247], v[252:255], v[212:215]
  ds_read_b64_tr_b16 a[84:85], v43 offset:32768
  v_mfma_f32_16x16x32_bf16 v[216:219], a[200:203], v[150:153], v[216:219]
  buffer_load_dwordx4 v37, s[40:43], s73 offen offset:3072 lds
  v_mfma_f32_16x16x32_bf16 v[216:219], a[216:219], v[154:157], v[216:219]
  ds_read_b64_tr_b16 a[86:87], v43 offset:34816
  ds_read_b64_tr_b16 a[88:89], v44 offset:32768
  v_mfma_f32_16x16x32_bf16 v[216:219], a[232:235], v[166:169], v[216:219]
  ds_read_b64_tr_b16 a[90:91], v44 offset:34816
  ds_read_b64_tr_b16 a[92:93], v45 offset:32768
  v_mfma_f32_16x16x32_bf16 v[216:219], a[248:251], v[252:255], v[216:219]
  ds_read_b64_tr_b16 a[94:95], v45 offset:34816
  v_mfma_f32_16x16x32_bf16 v[220:223], a[204:207], v[150:153], v[220:223]
  v_mfma_f32_16x16x32_bf16 v[220:223], a[220:223], v[154:157], v[220:223]
  v_mfma_f32_16x16x32_bf16 v[220:223], a[236:239], v[166:169], v[220:223]
  v_mfma_f32_16x16x32_bf16 v[220:223], a[252:255], v[252:255], v[220:223]
  s_add_u32 s5, s19, 0xe000
  v_cvt_pk_bf16_f32 v244, v208, v209
  v_cvt_pk_bf16_f32 v245, v210, v211
  v_cvt_pk_bf16_f32 v246, v212, v213
  v_cvt_pk_bf16_f32 v247, v214, v215
  v_pk_max_i16 v244, v244, 0
  v_pk_max_i16 v245, v245, 0
  v_pk_max_i16 v246, v246, 0
  v_pk_max_i16 v247, v247, 0
  buffer_store_dwordx4 v[244:247], v13, s[8:11], s5 offen sc1
  v_cvt_pk_bf16_f32 v248, v216, v217
  v_cvt_pk_bf16_f32 v249, v218, v219
  v_cvt_pk_bf16_f32 v250, v220, v221
  v_cvt_pk_bf16_f32 v251, v222, v223
  v_pk_max_i16 v248, v248, 0
  v_pk_max_i16 v249, v249, 0
  v_pk_max_i16 v250, v250, 0
  v_pk_max_i16 v251, v251, 0
  buffer_store_dwordx4 v[248:251], v13, s[8:11], s5 offen offset:1024 sc1
  s_waitcnt vmcnt(5)
  v_cmp_ne_u64 vcc, s[66:67], v[20:21]
  s_cmp_lg_u64 vcc, 0
  s_cbranch_scc1 L13_%=
L14_%=:
  s_add_u32 s51, s51, 0x8000
  s_add_u32 s77, s78, 0
  buffer_load_dwordx4 a[160:163], v2, s[8:11], s77 offen sc1
  buffer_load_dwordx4 a[164:167], v2, s[8:11], s77 offen offset:1024 sc1
  buffer_load_dwordx4 a[168:171], v2, s[8:11], s77 offen offset:2048 sc1
  buffer_load_dwordx4 a[172:175], v2, s[8:11], s77 offen offset:3072 sc1
  s_add_u32 s77, s78, 0x1000
  buffer_load_dwordx4 a[176:179], v2, s[8:11], s77 offen sc1
  buffer_load_dwordx4 a[180:183], v2, s[8:11], s77 offen offset:1024 sc1
  buffer_load_dwordx4 a[184:187], v2, s[8:11], s77 offen offset:2048 sc1
  buffer_load_dwordx4 a[188:191], v2, s[8:11], s77 offen offset:3072 sc1
  s_add_u32 s77, s78, 0x2000
  buffer_load_dwordx4 a[192:195], v2, s[8:11], s77 offen sc1
  ds_read_b64_tr_b16 a[96:97], v42 offset:20480
  ds_read_b64_tr_b16 a[98:99], v42 offset:22528
  ds_read_b64_tr_b16 a[100:101], v43 offset:20480
  ds_read_b64_tr_b16 a[102:103], v43 offset:22528
  ds_read_b64_tr_b16 a[104:105], v44 offset:20480
  ds_read_b64_tr_b16 a[106:107], v44 offset:22528
  ds_read_b64_tr_b16 a[108:109], v45 offset:20480
  ds_read_b64_tr_b16 a[110:111], v45 offset:22528
  ds_read_b64_tr_b16 a[112:113], v42 offset:24576
  ds_read_b64_tr_b16 a[114:115], v42 offset:26624
  ds_read_b64_tr_b16 a[116:117], v43 offset:24576
  ds_read_b64_tr_b16 a[118:119], v43 offset:26624
  ds_read_b64_tr_b16 a[120:121], v44 offset:24576
  ds_read_b64_tr_b16 a[122:123], v44 offset:26624
  ds_read_b64_tr_b16 a[124:125], v45 offset:24576
  ds_read_b64_tr_b16 a[126:127], v45 offset:26624
  v_mul_f32 v52, v16, v22
  ds_read_b64_tr_b16 a[128:129], v42 offset:16384
  v_mul_f32 v53, v16, v23
  ds_read_b64_tr_b16 a[130:131], v42 offset:18432
  v_mul_f32 v54, v16, v24
  ds_read_b64_tr_b16 a[132:133], v43 offset:16384
  v_mul_f32 v55, v16, v25
  ds_read_b64_tr_b16 a[134:135], v43 offset:18432
  v_mul_f32 v56, v16, v26
  ds_read_b64_tr_b16 a[136:137], v44 offset:16384
  v_mul_f32 v57, v16, v27
  ds_read_b64_tr_b16 a[138:139], v44 offset:18432
  v_mul_f32 v58, v16, v28
  ds_read_b64_tr_b16 a[140:141], v45 offset:16384
  v_mul_f32 v59, v16, v29
  ds_read_b64_tr_b16 a[142:143], v45 offset:18432
  v_mul_f32 v60, v17, v22
  s_waitcnt vmcnt(11)
  ds_read_b64_tr_b16 a[144:145], v42 offset:12288
  v_mul_f32 v61, v17, v23
  ds_read_b64_tr_b16 a[146:147], v42 offset:14336
  v_mul_f32 v62, v17, v24
  ds_read_b64_tr_b16 a[148:149], v43 offset:12288
  v_mul_f32 v63, v17, v25
  ds_read_b64_tr_b16 a[150:151], v43 offset:14336
  v_mul_f32 v64, v17, v26
  ds_read_b64_tr_b16 a[152:153], v44 offset:12288
  v_mul_f32 v65, v17, v27
  ds_read_b64_tr_b16 a[154:155], v44 offset:14336
  v_mul_f32 v66, v17, v28
  ds_read_b64_tr_b16 a[156:157], v45 offset:12288
  v_mul_f32 v67, v17, v29
  ds_read_b64_tr_b16 a[158:159], v45 offset:14336
  s_waitcnt vmcnt(8)
  v_mfma_f32_16x16x32_bf16 v[192:195], v[78:81], a[160:163], 0
  buffer_load_dwordx4 a[196:199], v2, s[8:11], s77 offen offset:1024 sc1
  v_mfma_f32_16x16x32_bf16 v[196:199], v[98:101], a[160:163], 0
  v_mfma_f32_16x16x32_bf16 v[200:203], v[102:105], a[160:163], 0
  buffer_load_dwordx4 a[200:203], v2, s[8:11], s77 offen offset:2048 sc1
  v_mfma_f32_16x16x32_bf16 v[204:207], v[106:109], a[160:163], 0
  s_waitcnt vmcnt(9)
  v_mfma_f32_16x16x32_bf16 v[192:195], v[110:113], a[164:167], v[192:195]
  buffer_load_dwordx4 a[204:207], v2, s[8:11], s77 offen offset:3072 sc1
  v_mfma_f32_16x16x32_bf16 v[196:199], v[114:117], a[164:167], v[196:199]
  v_mfma_f32_16x16x32_bf16 v[200:203], v[118:121], a[164:167], v[200:203]
LS25_%=:
  s_cbranch_execz L4_%=
  v_mfma_f32_16x16x32_bf16 v[204:207], v[122:125], a[164:167], v[204:207]
  s_waitcnt vmcnt(9)
  v_mfma_f32_16x16x32_bf16 v[192:195], v[126:129], a[168:171], v[192:195]
  s_add_u32 s77, s78, 0x3000
  s_waitcnt vmcnt(8)
  v_mfma_f32_16x16x32_bf16 v[192:195], v[142:145], a[172:175], v[192:195]
  s_barrier
  buffer_store_dwordx2 v[18:19], v9, s[8:11], s50 offen sc1
  s_add_u32 s50, s50, 0x8000
  s_waitcnt vmcnt(8)
  v_mfma_f32_16x16x32_bf16 v[192:195], a[0:3], a[176:179], v[192:195]
  buffer_load_dwordx4 a[208:211], v2, s[8:11], s77 offen sc1
  s_waitcnt vmcnt(8)
  v_mfma_f32_16x16x32_bf16 v[192:195], a[16:19], a[180:183], v[192:195]
  v_mfma_f32_16x16x32_bf16 v[196:199], v[130:133], a[168:171], v[196:199]
  v_mfma_f32_16x16x32_bf16 v[196:199], v[146:149], a[172:175], v[196:199]
  buffer_load_dwordx4 a[212:215], v2, s[8:11], s77 offen offset:1024 sc1
  v_mfma_f32_16x16x32_bf16 v[196:199], a[4:7], a[176:179], v[196:199]
  v_mfma_f32_16x16x32_bf16 v[196:199], a[20:23], a[180:183], v[196:199]
  v_mfma_f32_16x16x32_bf16 v[200:203], v[134:137], a[168:171], v[200:203]
  s_add_u32 s77, s79, 0
  v_mfma_f32_16x16x32_bf16 v[200:203], v[158:161], a[172:175], v[200:203]
  v_mfma_f32_16x16x32_bf16 v[200:203], a[8:11], a[176:179], v[200:203]
  buffer_load_dwordx4 a[216:219], v2, s[8:11], s77 offen sc1
  v_mfma_f32_16x16x32_bf16 v[200:203], a[24:27], a[180:183], v[200:203]
  v_mfma_f32_16x16x32_bf16 v[204:207], v[138:141], a[168:171], v[204:207]
  v_mfma_f32_16x16x32_bf16 v[204:207], v[162:165], a[172:175], v[204:207]
  buffer_load_dwordx4 a[220:223], v2, s[8:11], s77 offen offset:1024 sc1
  v_mfma_f32_16x16x32_bf16 v[204:207], a[12:15], a[176:179], v[204:207]
  v_mfma_f32_16x16x32_bf16 v[204:207], a[28:31], a[180:183], v[204:207]
  s_waitcnt vmcnt(10)
  v_mfma_f32_16x16x32_bf16 v[192:195], a[32:35], a[184:187], v[192:195]
  buffer_load_dwordx4 a[224:227], v2, s[8:11], s77 offen offset:2048 sc1
  s_waitcnt vmcnt(10)
  v_mfma_f32_16x16x32_bf16 v[192:195], a[48:51], a[188:191], v[192:195]
  s_waitcnt vmcnt(9) lgkmcnt(14)
  v_mfma_f32_16x16x32_bf16 v[192:195], a[64:67], a[192:195], v[192:195]
  s_waitcnt vmcnt(8)
  v_mfma_f32_16x16x32_bf16 v[192:195], a[80:83], a[196:199], v[192:195]
  buffer_load_dwordx4 a[228:231], v2, s[8:11], s77 offen offset:3072 sc1
  v_mfma_f32_16x16x32_bf16 v[196:199], a[36:39], a[184:187], v[196:199]
  v_mfma_f32_16x16x32_bf16 v[196:199], a[52:55], a[188:191], v[196:199]
  v_mfma_f32_16x16x32_bf16 v[196:199], a[68:71], a[192:195], v[196:199]
  s_add_u32 s77, s79, 0x1000
  v_mfma_f32_16x16x32_bf16 v[196:199], a[84:87], a[196:199], v[196:199]
  v_mfma_f32_16x16x32_bf16 v[200:203], a[40:43], a[184:187], v[200:203]
  v_mfma_f32_16x16x32_bf16 v[200:203], a[56:59], a[188:191], v[200:203]
  buffer_load_dwordx4 a[232:235], v2, s[8:11], s77 offen sc1
  v_mfma_f32_16x16x32_bf16 v[200:203], a[72:75], a[192:195], v[200:203]
  v_mfma_f32_16x16x32_bf16 v[200:203], a[88:91], a[196:199], v[200:203]
  v_mfma_f32_16x16x32_bf16 v[204:207], a[44:47], a[184:187], v[204:207]
  buffer_load_dwordx4 a[236:239], v2, s[8:11], s77 offen offset:1024 sc1
  v_mfma_f32_16x16x32_bf16 v[204:207], a[60:63], a[188:191], v[204:207]
  v_mfma_f32_16x16x32_bf16 v[204:207], a[76:79], a[192:195], v[204:207]
  v_mfma_f32_16x16x32_bf16 v[204:207], a[92:95], a[196:199], v[204:207]
  s_waitcnt vmcnt(10)
  v_mfma_f32_16x16x32_bf16 v[192:195], a[96:99], a[200:203], v[192:195]
  buffer_load_dwordx4 a[160:163], v2, s[8:11], s77 offen offset:2048 sc1
  s_waitcnt vmcnt(10)
  v_mfma_f32_16x16x32_bf16 v[192:195], a[112:115], a[204:207], v[192:195]
  s_waitcnt vmcnt(8)
  v_mfma_f32_16x16x32_bf16 v[192:195], a[128:131], a[208:211], v[192:195]
  s_waitcnt vmcnt(7) lgkmcnt(6)
  v_mfma_f32_16x16x32_bf16 v[192:195], a[144:147], a[212:215], v[192:195]
  buffer_load_dwordx4 a[164:167], v2, s[8:11], s77 offen offset:3072 sc1
  v_mfma_f32_16x16x32_bf16 v[196:199], a[100:103], a[200:203], v[196:199]
  v_mfma_f32_16x16x32_bf16 v[196:199], a[116:119], a[204:207], v[196:199]
  v_mfma_f32_16x16x32_bf16 v[196:199], a[132:135], a[208:211], v[196:199]
  s_add_u32 s77, s79, 0x2000
  s_waitcnt lgkmcnt(4)
  v_mfma_f32_16x16x32_bf16 v[196:199], a[148:151], a[212:215], v[196:199]
  v_mfma_f32_16x16x32_bf16 v[200:203], a[104:107], a[200:203], v[200:203]
  v_mfma_f32_16x16x32_bf16 v[200:203], a[120:123], a[204:207], v[200:203]
  buffer_load_dwordx4 a[168:171], v2, s[8:11], s77 offen sc1
  v_mfma_f32_16x16x32_bf16 v[200:203], a[136:139], a[208:211], v[200:203]
  s_waitcnt lgkmcnt(2)
  v_mfma_f32_16x16x32_bf16 v[200:203], a[152:155], a[212:215], v[200:203]
  v_mfma_f32_16x16x32_bf16 v[204:207], a[108:111], a[200:203], v[204:207]
  buffer_load_dwordx4 a[172:175], v2, s[8:11], s77 offen offset:1024 sc1
  v_mfma_f32_16x16x32_bf16 v[204:207], a[124:127], a[204:207], v[204:207]
  v_mfma_f32_16x16x32_bf16 v[204:207], a[140:143], a[208:211], v[204:207]
  s_waitcnt lgkmcnt(0)
  v_mfma_f32_16x16x32_bf16 v[204:207], a[156:159], a[212:215], v[204:207]
  s_waitcnt vmcnt(9)
  v_mfma_f32_16x16x32_bf16 v[208:211], v[78:81], a[216:219], 0
  buffer_load_dwordx4 a[176:179], v2, s[8:11], s77 offen offset:2048 sc1
  s_waitcnt vmcnt(9)
  v_mfma_f32_16x16x32_bf16 v[208:211], v[110:113], a[220:223], v[208:211]
  s_waitcnt vmcnt(8)
  v_mfma_f32_16x16x32_bf16 v[208:211], v[126:129], a[224:227], v[208:211]
  buffer_load_dwordx4 a[180:183], v2, s[8:11], s77 offen offset:3072 sc1
  s_waitcnt vmcnt(8)
  v_mfma_f32_16x16x32_bf16 v[208:211], v[142:145], a[228:231], v[208:211]
  ds_write_b128 v11, v[192:195]
  v_mfma_f32_16x16x32_bf16 v[212:215], v[98:101], a[216:219], 0
  v_mfma_f32_16x16x32_bf16 v[212:215], v[114:117], a[220:223], v[212:215]
  s_add_u32 s77, s79, 0x3000
  v_mfma_f32_16x16x32_bf16 v[212:215], v[130:133], a[224:227], v[212:215]
  ds_write_b128 v11, v[196:199] offset:1024
  v_mfma_f32_16x16x32_bf16 v[212:215], v[146:149], a[228:231], v[212:215]
  v_mfma_f32_16x16x32_bf16 v[216:219], v[102:105], a[216:219], 0
  buffer_load_dwordx4 a[184:187], v2, s[8:11], s77 offen sc1
  v_mfma_f32_16x16x32_bf16 v[216:219], v[118:121], a[220:223], v[216:219]
  ds_write_b128 v11, v[200:203] offset:2048
  v_mfma_f32_16x16x32_bf16 v[216:219], v[134:137], a[224:227], v[216:219]
  buffer_load_dwordx4 a[188:191], v2, s[8:11], s77 offen offset:1024 sc1
  v_mfma_f32_16x16x32_bf16 v[216:219], v[158:161], a[228:231], v[216:219]
  v_mfma_f32_16x16x32_bf16 v[220:223], v[106:109], a[216:219], 0
  ds_write_b128 v11, v[204:207] offset:3072
  v_mfma_f32_16x16x32_bf16 v[220:223], v[122:125], a[220:223], v[220:223]
  buffer_load_dwordx2 v[20:21], v12, s[8:11], s51 offen sc1
  v_mfma_f32_16x16x32_bf16 v[220:223], v[138:141], a[224:227], v[220:223]
  v_mfma_f32_16x16x32_bf16 v[220:223], v[162:165], a[228:231], v[220:223]
  s_waitcnt vmcnt(10)
  v_mfma_f32_16x16x32_bf16 v[208:211], a[0:3], a[232:235], v[208:211]
  s_waitcnt vmcnt(9)
  v_mfma_f32_16x16x32_bf16 v[208:211], a[16:19], a[236:239], v[208:211]
  s_waitcnt vmcnt(8)
  v_mfma_f32_16x16x32_bf16 v[208:211], a[32:35], a[160:163], v[208:211]
  s_waitcnt vmcnt(7)
  v_mfma_f32_16x16x32_bf16 v[208:211], a[48:51], a[164:167], v[208:211]
  v_mfma_f32_16x16x32_bf16 v[212:215], a[4:7], a[232:235], v[212:215]
  v_mfma_f32_16x16x32_bf16 v[212:215], a[20:23], a[236:239], v[212:215]
  v_mfma_f32_16x16x32_bf16 v[212:215], a[36:39], a[160:163], v[212:215]
  v_mfma_f32_16x16x32_bf16 v[212:215], a[52:55], a[164:167], v[212:215]
  v_mfma_f32_16x16x32_bf16 v[216:219], a[8:11], a[232:235], v[216:219]
  v_mfma_f32_16x16x32_bf16 v[216:219], a[24:27], a[236:239], v[216:219]
  v_mfma_f32_16x16x32_bf16 v[216:219], a[40:43], a[160:163], v[216:219]
  v_mfma_f32_16x16x32_bf16 v[216:219], a[56:59], a[164:167], v[216:219]
  v_mfma_f32_16x16x32_bf16 v[220:223], a[12:15], a[232:235], v[220:223]
  v_mfma_f32_16x16x32_bf16 v[220:223], a[28:31], a[236:239], v[220:223]
  v_mfma_f32_16x16x32_bf16 v[220:223], a[44:47], a[160:163], v[220:223]
  v_mfma_f32_16x16x32_bf16 v[220:223], a[60:63], a[164:167], v[220:223]
  s_waitcnt vmcnt(6)
  v_mfma_f32_16x16x32_bf16 v[208:211], a[64:67], a[168:171], v[208:211]
  s_waitcnt vmcnt(0)
  v_cmp_ne_u64 vcc, s[66:67], v[20:21]
  s_cmp_lg_u64 vcc, 0
  s_cbranch_scc1 L15_%=
L16_%=:
  s_add_u32 s51, s51, 0x8000
  v_mfma_f32_16x16x32_bf16 v[208:211], a[80:83], a[172:175], v[208:211]
  s_add_u32 s77, s78, 0x1c000
LS26_%=:
  s_cbranch_execz L4_%=
  v_mfma_f32_16x16x32_bf16 v[208:211], a[96:99], a[176:179], v[208:211]
  buffer_load_dwordx4 a[192:195], v2, s[8:11], s77 offen sc1
  v_mfma_f32_16x16x32_bf16 v[212:215], a[68:71], a[168:171], v[212:215]
  buffer_load_dwordx4 a[196:199], v2, s[8:11], s77 offen offset:1024 sc1
  v_mfma_f32_16x16x32_bf16 v[212:215], a[84:87], a[172:175], v[212:215]
  buffer_load_dwordx4 a[200:203], v2, s[8:11], s77 offen offset:2048 sc1
  v_mfma_f32_16x16x32_bf16 v[212:215], a[100:103], a[176:179], v[212:215]
  v_mfma_f32_16x16x32_bf16 v[216:219], a[72:75], a[168:171], v[216:219]
  buffer_load_dwordx4 a[204:207], v2, s[8:11], s77 offen offset:3072 sc1
  v_mfma_f32_16x16x32_bf16 v[216:219], a[88:91], a[172:175], v[216:219]
  s_add_u32 s77, s78, 0x1d000
  v_mfma_f32_16x16x32_bf16 v[216:219], a[104:107], a[176:179], v[216:219]
  buffer_load_dwordx4 a[208:211], v2, s[8:11], s77 offen sc1
  v_mfma_f32_16x16x32_bf16 v[220:223], a[76:79], a[168:171], v[220:223]
  buffer_load_dwordx4 a[212:215], v2, s[8:11], s77 offen offset:1024 sc1
  v_mfma_f32_16x16x32_bf16 v[220:223], a[92:95], a[172:175], v[220:223]
  buffer_load_dwordx4 a[216:219], v2, s[8:11], s77 offen offset:2048 sc1
  v_mfma_f32_16x16x32_bf16 v[220:223], a[108:111], a[176:179], v[220:223]
  v_mfma_f32_16x16x32_bf16 v[208:211], a[112:115], a[180:183], v[208:211]
  buffer_load_dwordx4 a[220:223], v2, s[8:11], s77 offen offset:3072 sc1
  v_mfma_f32_16x16x32_bf16 v[208:211], a[128:131], a[184:187], v[208:211]
  v_mfma_f32_16x16x32_bf16 v[208:211], a[144:147], a[188:191], v[208:211]
  v_mfma_f32_16x16x32_bf16 v[212:215], a[116:119], a[180:183], v[212:215]
  s_add_u32 s77, s78, 0x1e000
  v_mfma_f32_16x16x32_bf16 v[212:215], a[132:135], a[184:187], v[212:215]
  v_mfma_f32_16x16x32_bf16 v[212:215], a[148:151], a[188:191], v[212:215]
  v_mfma_f32_16x16x32_bf16 v[216:219], a[120:123], a[180:183], v[216:219]
  buffer_load_dwordx4 a[224:227], v2, s[8:11], s77 offen sc1
  v_mfma_f32_16x16x32_bf16 v[216:219], a[136:139], a[184:187], v[216:219]
  v_mfma_f32_16x16x32_bf16 v[216:219], a[152:155], a[188:191], v[216:219]
  v_mfma_f32_16x16x32_bf16 v[220:223], a[124:127], a[180:183], v[220:223]
  buffer_load_dwordx4 a[228:231], v2, s[8:11], s77 offen offset:1024 sc1
  v_mfma_f32_16x16x32_bf16 v[220:223], a[140:143], a[184:187], v[220:223]
  v_mfma_f32_16x16x32_bf16 v[220:223], a[156:159], a[188:191], v[220:223]
  s_waitcnt vmcnt(9)
  v_mfma_f32_16x16x32_bf16 v[224:227], v[78:81], a[192:195], 0
  buffer_load_dwordx4 a[232:235], v2, s[8:11], s77 offen offset:2048 sc1
  s_waitcnt vmcnt(9)
  v_mfma_f32_16x16x32_bf16 v[224:227], v[110:113], a[196:199], v[224:227]
  s_waitcnt vmcnt(8)
  v_mfma_f32_16x16x32_bf16 v[224:227], v[126:129], a[200:203], v[224:227]
  s_waitcnt vmcnt(7)
  v_mfma_f32_16x16x32_bf16 v[224:227], v[142:145], a[204:207], v[224:227]
  buffer_load_dwordx4 a[236:239], v2, s[8:11], s77 offen offset:3072 sc1
  v_mfma_f32_16x16x32_bf16 v[228:231], v[98:101], a[192:195], 0
  ds_write_b128 v11, v[208:211] offset:4096
  v_mfma_f32_16x16x32_bf16 v[228:231], v[114:117], a[196:199], v[228:231]
  ds_write_b128 v11, v[212:215] offset:5120
  v_mfma_f32_16x16x32_bf16 v[228:231], v[130:133], a[200:203], v[228:231]
  s_add_u32 s77, s78, 0x1f000
  v_mfma_f32_16x16x32_bf16 v[228:231], v[146:149], a[204:207], v[228:231]
  v_mfma_f32_16x16x32_bf16 v[232:235], v[102:105], a[192:195], 0
  v_mfma_f32_16x16x32_bf16 v[232:235], v[118:121], a[196:199], v[232:235]
  buffer_load_dwordx4 a[160:163], v2, s[8:11], s77 offen sc1
  v_mfma_f32_16x16x32_bf16 v[232:235], v[134:137], a[200:203], v[232:235]
  v_mfma_f32_16x16x32_bf16 v[232:235], v[158:161], a[204:207], v[232:235]
  v_mfma_f32_16x16x32_bf16 v[236:239], v[106:109], a[192:195], 0
  buffer_load_dwordx4 a[164:167], v2, s[8:11], s77 offen offset:1024 sc1
  v_mfma_f32_16x16x32_bf16 v[236:239], v[122:125], a[196:199], v[236:239]
  v_mfma_f32_16x16x32_bf16 v[236:239], v[138:141], a[200:203], v[236:239]
  s_waitcnt lgkmcnt(0)
  s_barrier
  v_mfma_f32_16x16x32_bf16 v[236:239], v[162:165], a[204:207], v[236:239]
  s_waitcnt vmcnt(9)
  v_mfma_f32_16x16x32_bf16 v[224:227], a[0:3], a[208:211], v[224:227]
  s_add_u32 s77, s79, 0x1c000
  ds_read_b128 v[192:195], v46
  s_waitcnt vmcnt(8)
  v_mfma_f32_16x16x32_bf16 v[224:227], a[16:19], a[212:215], v[224:227]
  s_waitcnt vmcnt(7)
  v_mfma_f32_16x16x32_bf16 v[224:227], a[32:35], a[216:219], v[224:227]
  ds_read_b128 v[196:199], v46 offset:1024
  s_waitcnt vmcnt(6)
  v_mfma_f32_16x16x32_bf16 v[224:227], a[48:51], a[220:223], v[224:227]
  buffer_load_dwordx4 a[168:171], v2, s[8:11], s77 offen sc1
  v_mfma_f32_16x16x32_bf16 v[228:231], a[4:7], a[208:211], v[228:231]
  ds_read_b128 v[200:203], v47 offset:2048
  v_mfma_f32_16x16x32_bf16 v[228:231], a[20:23], a[212:215], v[228:231]
  v_mfma_f32_16x16x32_bf16 v[228:231], a[36:39], a[216:219], v[228:231]
  buffer_load_dwordx4 a[172:175], v2, s[8:11], s77 offen offset:1024 sc1
  v_mfma_f32_16x16x32_bf16 v[228:231], a[52:55], a[220:223], v[228:231]
  ds_read_b128 v[204:207], v47 offset:3072
  v_mfma_f32_16x16x32_bf16 v[232:235], a[8:11], a[208:211], v[232:235]
  v_mfma_f32_16x16x32_bf16 v[232:235], a[24:27], a[212:215], v[232:235]
  buffer_load_dwordx4 a[176:179], v2, s[8:11], s77 offen offset:2048 sc1
  v_mfma_f32_16x16x32_bf16 v[232:235], a[40:43], a[216:219], v[232:235]
  ds_read_b128 v[208:211], v48 offset:4096
  v_mfma_f32_16x16x32_bf16 v[232:235], a[56:59], a[220:223], v[232:235]
  ds_read_b128 v[212:215], v48 offset:5120
  v_mfma_f32_16x16x32_bf16 v[236:239], a[12:15], a[208:211], v[236:239]
  buffer_load_dwordx4 a[180:183], v2, s[8:11], s77 offen offset:3072 sc1
  v_mfma_f32_16x16x32_bf16 v[236:239], a[28:31], a[212:215], v[236:239]
  s_waitcnt lgkmcnt(5)
  v_add_f32 v216, v216, v192
  v_mfma_f32_16x16x32_bf16 v[236:239], a[44:47], a[216:219], v[236:239]
  v_mfma_f32_16x16x32_bf16 v[236:239], a[60:63], a[220:223], v[236:239]
  s_waitcnt vmcnt(9)
  v_mfma_f32_16x16x32_bf16 v[224:227], a[64:67], a[224:227], v[224:227]
  s_add_u32 s77, s79, 0x1d000
  v_add_f32 v217, v217, v193
  s_waitcnt vmcnt(8)
  v_mfma_f32_16x16x32_bf16 v[224:227], a[80:83], a[228:231], v[224:227]
  v_add_f32 v218, v218, v194
  s_waitcnt vmcnt(7)
  v_mfma_f32_16x16x32_bf16 v[224:227], a[96:99], a[232:235], v[224:227]
  v_mfma_f32_16x16x32_bf16 v[228:231], a[68:71], a[224:227], v[228:231]
  buffer_load_dwordx4 a[184:187], v2, s[8:11], s77 offen sc1
  v_mfma_f32_16x16x32_bf16 v[228:231], a[84:87], a[228:231], v[228:231]
  v_add_f32 v219, v219, v195
  v_mfma_f32_16x16x32_bf16 v[228:231], a[100:103], a[232:235], v[228:231]
  s_waitcnt lgkmcnt(4)
  v_add_f32 v220, v220, v196
  v_mfma_f32_16x16x32_bf16 v[232:235], a[72:75], a[224:227], v[232:235]
  buffer_load_dwordx4 a[188:191], v2, s[8:11], s77 offen offset:1024 sc1
  v_mfma_f32_16x16x32_bf16 v[232:235], a[88:91], a[228:231], v[232:235]
  v_add_f32 v221, v221, v197
  v_mfma_f32_16x16x32_bf16 v[232:235], a[104:107], a[232:235], v[232:235]
  v_add_f32 v222, v222, v198
  v_mfma_f32_16x16x32_bf16 v[236:239], a[76:79], a[224:227], v[236:239]
  buffer_load_dwordx4 a[192:195], v2, s[8:11], s77 offen offset:2048 sc1
  v_mfma_f32_16x16x32_bf16 v[236:239], a[92:95], a[228:231], v[236:239]
  v_add_f32 v223, v223, v199
  v_mfma_f32_16x16x32_bf16 v[236:239], a[108:111], a[232:235], v[236:239]
  s_waitcnt vmcnt(9)
  v_mfma_f32_16x16x32_bf16 v[224:227], a[112:115], a[236:239], v[224:227]
  buffer_load_dwordx4 a[196:199], v2, s[8:11], s77 offen offset:3072 sc1
  s_waitcnt vmcnt(9)
  v_mfma_f32_16x16x32_bf16 v[224:227], a[128:131], a[160:163], v[224:227]
  s_waitcnt lgkmcnt(3)
  v_add_f32 v216, v216, v200
  v_add_f32 v217, v217, v201
  s_waitcnt vmcnt(8)
  v_mfma_f32_16x16x32_bf16 v[224:227], a[144:147], a[164:167], v[224:227]
  v_mfma_f32_16x16x32_bf16 v[228:231], a[116:119], a[236:239], v[228:231]
  s_add_u32 s77, s79, 0x1e000
  v_add_f32 v218, v218, v202
  v_mfma_f32_16x16x32_bf16 v[228:231], a[132:135], a[160:163], v[228:231]
  v_mfma_f32_16x16x32_bf16 v[228:231], a[148:151], a[164:167], v[228:231]
  v_add_f32 v219, v219, v203
  v_mfma_f32_16x16x32_bf16 v[232:235], a[120:123], a[236:239], v[232:235]
  buffer_load_dwordx4 a[200:203], v2, s[8:11], s77 offen sc1
  v_mfma_f32_16x16x32_bf16 v[232:235], a[136:139], a[160:163], v[232:235]
  s_waitcnt lgkmcnt(2)
  v_add_f32 v220, v220, v204
LS27_%=:
  s_cbranch_execz L4_%=
  v_mfma_f32_16x16x32_bf16 v[232:235], a[152:155], a[164:167], v[232:235]
  v_add_f32 v221, v221, v205
  v_mfma_f32_16x16x32_bf16 v[236:239], a[124:127], a[236:239], v[236:239]
  buffer_load_dwordx4 a[204:207], v2, s[8:11], s77 offen offset:1024 sc1
  v_mfma_f32_16x16x32_bf16 v[236:239], a[140:143], a[160:163], v[236:239]
  v_add_f32 v222, v222, v206
  v_mfma_f32_16x16x32_bf16 v[236:239], a[156:159], a[164:167], v[236:239]
  s_waitcnt vmcnt(9)
  v_mfma_f32_16x16x32_bf16 v[240:243], v[78:81], a[168:171], 0
  buffer_load_dwordx4 a[208:211], v2, s[8:11], s77 offen offset:2048 sc1
  s_waitcnt vmcnt(9)
  v_mfma_f32_16x16x32_bf16 v[240:243], v[110:113], a[172:175], v[240:243]
  v_add_f32 v223, v223, v207
  s_waitcnt vmcnt(8)
  v_mfma_f32_16x16x32_bf16 v[240:243], v[126:129], a[176:179], v[240:243]
  s_waitcnt lgkmcnt(1)
  v_add_f32 v216, v216, v208
  s_waitcnt vmcnt(7)
  v_mfma_f32_16x16x32_bf16 v[240:243], v[142:145], a[180:183], v[240:243]
  buffer_load_dwordx4 a[212:215], v2, s[8:11], s77 offen offset:3072 sc1
  v_mfma_f32_16x16x32_bf16 v[244:247], v[98:101], a[168:171], 0
  ds_write_b128 v11, v[224:227] offset:6144
  v_mfma_f32_16x16x32_bf16 v[244:247], v[114:117], a[172:175], v[244:247]
  v_add_f32 v217, v217, v209
  v_mfma_f32_16x16x32_bf16 v[244:247], v[130:133], a[176:179], v[244:247]
  s_add_u32 s77, s79, 0x1f000
  v_add_f32 v218, v218, v210
  v_mfma_f32_16x16x32_bf16 v[244:247], v[146:149], a[180:183], v[244:247]
  ds_write_b128 v11, v[228:231] offset:7168
  v_mfma_f32_16x16x32_bf16 v[248:251], v[102:105], a[168:171], 0
  v_mfma_f32_16x16x32_bf16 v[248:251], v[118:121], a[172:175], v[248:251]
  buffer_load_dwordx4 a[216:219], v2, s[8:11], s77 offen sc1
  v_mfma_f32_16x16x32_bf16 v[248:251], v[134:137], a[176:179], v[248:251]
  ds_write_b128 v11, v[232:235] offset:8192
  v_mfma_f32_16x16x32_bf16 v[248:251], v[158:161], a[180:183], v[248:251]
  v_add_f32 v219, v219, v211
  s_waitcnt lgkmcnt(3)
  v_add_f32 v220, v220, v212
  v_mfma_f32_16x16x32_bf16 v[252:255], v[106:109], a[168:171], 0
  buffer_load_dwordx4 a[220:223], v2, s[8:11], s77 offen offset:1024 sc1
  v_mfma_f32_16x16x32_bf16 v[252:255], v[122:125], a[172:175], v[252:255]
  ds_write_b128 v11, v[236:239] offset:9216
  v_mfma_f32_16x16x32_bf16 v[252:255], v[138:141], a[176:179], v[252:255]
  v_add_f32 v221, v221, v213
  v_mfma_f32_16x16x32_bf16 v[252:255], v[162:165], a[180:183], v[252:255]
  s_waitcnt vmcnt(9)
  v_mfma_f32_16x16x32_bf16 v[240:243], a[0:3], a[184:187], v[240:243]
  v_add_f32 v222, v222, v214
  s_waitcnt vmcnt(8)
  v_mfma_f32_16x16x32_bf16 v[240:243], a[16:19], a[188:191], v[240:243]
  s_waitcnt vmcnt(7)
  v_mfma_f32_16x16x32_bf16 v[240:243], a[32:35], a[192:195], v[240:243]
  v_add_f32 v223, v223, v215
  s_waitcnt vmcnt(6)
  v_mfma_f32_16x16x32_bf16 v[240:243], a[48:51], a[196:199], v[240:243]
  v_mfma_f32_16x16x32_bf16 v[244:247], a[4:7], a[184:187], v[244:247]
  v_fma_f32 v216, v216, v52, v176
  v_mfma_f32_16x16x32_bf16 v[244:247], a[20:23], a[188:191], v[244:247]
  v_mfma_f32_16x16x32_bf16 v[244:247], a[36:39], a[192:195], v[244:247]
  v_fma_f32 v217, v217, v53, v177
  v_mfma_f32_16x16x32_bf16 v[244:247], a[52:55], a[196:199], v[244:247]
  v_mfma_f32_16x16x32_bf16 v[248:251], a[8:11], a[184:187], v[248:251]
  v_mfma_f32_16x16x32_bf16 v[248:251], a[24:27], a[188:191], v[248:251]
  v_fma_f32 v218, v218, v54, v178
  v_mfma_f32_16x16x32_bf16 v[248:251], a[40:43], a[192:195], v[248:251]
  v_mfma_f32_16x16x32_bf16 v[248:251], a[56:59], a[196:199], v[248:251]
  v_fma_f32 v219, v219, v55, v179
  v_mfma_f32_16x16x32_bf16 v[252:255], a[12:15], a[184:187], v[252:255]
  v_mfma_f32_16x16x32_bf16 v[252:255], a[28:31], a[188:191], v[252:255]
  v_fma_f32 v220, v220, v56, v180
  v_mfma_f32_16x16x32_bf16 v[252:255], a[44:47], a[192:195], v[252:255]
  v_mfma_f32_16x16x32_bf16 v[252:255], a[60:63], a[196:199], v[252:255]
  s_waitcnt vmcnt(5)
  v_mfma_f32_16x16x32_bf16 v[240:243], a[64:67], a[200:203], v[240:243]
  v_fma_f32 v221, v221, v57, v181
  s_waitcnt vmcnt(4)
  v_mfma_f32_16x16x32_bf16 v[240:243], a[80:83], a[204:207], v[240:243]
  v_fma_f32 v222, v222, v58, v182
  s_waitcnt vmcnt(3)
  v_mfma_f32_16x16x32_bf16 v[240:243], a[96:99], a[208:211], v[240:243]
  v_mfma_f32_16x16x32_bf16 v[244:247], a[68:71], a[200:203], v[244:247]
  v_fma_f32 v223, v223, v59, v183
  v_mfma_f32_16x16x32_bf16 v[244:247], a[84:87], a[204:207], v[244:247]
  v_mfma_f32_16x16x32_bf16 v[244:247], a[100:103], a[208:211], v[244:247]
  v_cvt_pk_bf16_f32 v192, v216, v217
  v_mfma_f32_16x16x32_bf16 v[248:251], a[72:75], a[200:203], v[248:251]
  v_cvt_pk_bf16_f32 v193, v218, v219
  v_mfma_f32_16x16x32_bf16 v[248:251], a[88:91], a[204:207], v[248:251]
  v_mfma_f32_16x16x32_bf16 v[248:251], a[104:107], a[208:211], v[248:251]
  v_cvt_pk_bf16_f32 v194, v220, v221
  v_mfma_f32_16x16x32_bf16 v[252:255], a[76:79], a[200:203], v[252:255]
  v_mfma_f32_16x16x32_bf16 v[252:255], a[92:95], a[204:207], v[252:255]
  v_cvt_pk_bf16_f32 v195, v222, v223
  v_mfma_f32_16x16x32_bf16 v[252:255], a[108:111], a[208:211], v[252:255]
  s_waitcnt vmcnt(2)
  v_mfma_f32_16x16x32_bf16 v[240:243], a[112:115], a[212:215], v[240:243]
  v_permlane16_swap_b32 v192, v194
  s_waitcnt vmcnt(1)
  v_mfma_f32_16x16x32_bf16 v[240:243], a[128:131], a[216:219], v[240:243]
  s_waitcnt vmcnt(0)
  v_mfma_f32_16x16x32_bf16 v[240:243], a[144:147], a[220:223], v[240:243]
  v_mfma_f32_16x16x32_bf16 v[244:247], a[116:119], a[212:215], v[244:247]
  v_mfma_f32_16x16x32_bf16 v[244:247], a[132:135], a[216:219], v[244:247]
  v_permlane16_swap_b32 v193, v195
  v_mfma_f32_16x16x32_bf16 v[244:247], a[148:151], a[220:223], v[244:247]
  v_mfma_f32_16x16x32_bf16 v[248:251], a[120:123], a[212:215], v[248:251]
  v_mfma_f32_16x16x32_bf16 v[248:251], a[136:139], a[216:219], v[248:251]
  v_mfma_f32_16x16x32_bf16 v[248:251], a[152:155], a[220:223], v[248:251]
  buffer_store_dwordx4 v[192:195], v15, s[44:47], s74 offen
  v_mfma_f32_16x16x32_bf16 v[252:255], a[124:127], a[212:215], v[252:255]
  ds_write_b128 v11, v[240:243] offset:10240
  v_mfma_f32_16x16x32_bf16 v[252:255], a[140:143], a[216:219], v[252:255]
  s_nop 0
  ds_write_b128 v11, v[244:247] offset:11264
  v_mfma_f32_16x16x32_bf16 v[252:255], a[156:159], a[220:223], v[252:255]
  s_waitcnt lgkmcnt(0)
  s_barrier
  ds_read_b128 v[224:227], v46 offset:6144
  ds_read_b128 v[228:231], v46 offset:7168
  ds_read_b128 v[232:235], v47 offset:8192
  ds_read_b128 v[236:239], v47 offset:9216
  ds_read_b128 v[240:243], v48 offset:10240
  ds_read_b128 v[244:247], v48 offset:11264
  s_waitcnt lgkmcnt(5)
  v_pk_add_f32 v[248:249], v[248:249], v[224:225]
  v_pk_add_f32 v[250:251], v[250:251], v[226:227]
  s_waitcnt lgkmcnt(4)
  v_pk_add_f32 v[252:253], v[252:253], v[228:229]
  v_pk_add_f32 v[254:255], v[254:255], v[230:231]
  s_waitcnt lgkmcnt(3)
  v_pk_add_f32 v[248:249], v[248:249], v[232:233]
  v_pk_add_f32 v[250:251], v[250:251], v[234:235]
  s_waitcnt lgkmcnt(2)
  v_pk_add_f32 v[252:253], v[252:253], v[236:237]
  v_pk_add_f32 v[254:255], v[254:255], v[238:239]
  s_waitcnt lgkmcnt(1)
  v_pk_add_f32 v[248:249], v[248:249], v[240:241]
  v_pk_add_f32 v[250:251], v[250:251], v[242:243]
  s_waitcnt lgkmcnt(0)
  v_pk_add_f32 v[252:253], v[252:253], v[244:245]
  v_pk_add_f32 v[254:255], v[254:255], v[246:247]
  v_pk_fma_f32 v[248:249], v[248:249], v[60:61], v[184:185]
  v_pk_fma_f32 v[250:251], v[250:251], v[62:63], v[186:187]
  v_pk_fma_f32 v[252:253], v[252:253], v[64:65], v[188:189]
  v_pk_fma_f32 v[254:255], v[254:255], v[66:67], v[190:191]
  v_cvt_pk_bf16_f32 v224, v248, v249
  v_cvt_pk_bf16_f32 v225, v250, v251
  s_nop 0
  v_cvt_pk_bf16_f32 v226, v252, v253
  v_cvt_pk_bf16_f32 v227, v254, v255
  s_nop 0
  v_permlane16_swap_b32 v224, v226
  v_permlane16_swap_b32 v225, v227
  buffer_store_dwordx4 v[224:227], v15, s[44:47], s75 offen
  s_endpgm
L7_%=:
L18_%=:
  buffer_load_dwordx2 v[20:21], v77, s[8:11], s48 offen sc1
  s_waitcnt vmcnt(0)
  v_cmp_ne_u64 vcc, s[66:67], v[20:21]
  s_cmp_eq_u64 vcc, 0
  s_cbranch_scc1 L19_%=
  s_sub_u32 s64, s64, 1
  s_cbranch_scc0 L18_%=
  s_mov_b32 s64, 0
L19_%=:
  s_branch L8_%=
L9_%=:
L20_%=:
  buffer_load_dwordx2 v[20:21], v12, s[8:11], s51 offen sc1
  s_waitcnt vmcnt(0)
  v_cmp_ne_u64 vcc, s[66:67], v[20:21]
  s_cmp_eq_u64 vcc, 0
  s_cbranch_scc1 L21_%=
  s_sub_u32 s64, s64, 1
  s_cbranch_scc0 L20_%=
  s_mov_b32 s64, 0
L21_%=:
  s_branch L10_%=
L11_%=:
L22_%=:
  buffer_load_dwordx2 v[20:21], v12, s[8:11], s51 offen sc1
  s_waitcnt vmcnt(0)
  v_cmp_ne_u64 vcc, s[66:67], v[20:21]
  s_cmp_eq_u64 vcc, 0
  s_cbranch_scc1 L23_%=
  s_sub_u32 s64, s64, 1
  s_cbranch_scc0 L22_%=
  s_mov_b32 s64, 0
L23_%=:
  s_branch L12_%=
L13_%=:
L24_%=:
  buffer_load_dwordx2 v[20:21], v12, s[8:11], s51 offen sc1
  s_waitcnt vmcnt(0)
  v_cmp_ne_u64 vcc, s[66:67], v[20:21]
  s_cmp_eq_u64 vcc, 0
  s_cbranch_scc1 L25_%=
  s_sub_u32 s64, s64, 1
  s_cbranch_scc0 L24_%=
  s_mov_b32 s64, 0
L25_%=:
  s_branch L14_%=
L15_%=:
L26_%=:
  buffer_load_dwordx2 v[20:21], v12, s[8:11], s51 offen sc1
  s_waitcnt vmcnt(0)
  v_cmp_ne_u64 vcc, s[66:67], v[20:21]
  s_cmp_eq_u64 vcc, 0
  s_cbranch_scc1 L27_%=
  s_sub_u32 s64, s64, 1
  s_cbranch_scc0 L26_%=
  s_mov_b32 s64, 0
L27_%=:
  s_branch L16_%=
.p2align 12
L17_%=:
)ASM"
               :
               : "{s[0:1]}"(__builtin_amdgcn_kernarg_segment_ptr()), "{s2}"(blockIdx.x), "{v0}"(threadIdx.x),
                 "{s3}"((unsigned)(size_t)(__attribute__((address_space(3))) char*)lds)
               : "memory", "vcc", "scc", "v255", "a255", "s95");
#endif
}

extern "C" void kernel_launch(void* const* d_in, const int* in_sizes, int n_in, void* d_out, int out_size,
                              void* d_ws, size_t ws_size, hipStream_t stream) {
  const uint32_t F = 0x00020000u;
  Args a;
  a.d[0] = {d_in[0], 2048u * 512u * 2u, F};
  a.d[1] = {d_ws, 17825792u, F};
  a.d[2] = {d_in[9], 512u * 3072u * 2u, F};
  a.d[3] = {d_in[8], 512u * 2u, F};
  a.d[4] = {d_in[1], 2048u * 2048u * 2u, F};
  a.d[5] = {d_in[2], 2048u * 2048u * 2u, F};
  a.d[6] = {d_in[3], 512u * 512u * 2u, F};
  a.d[7] = {d_in[4], 512u * 512u * 2u, F};
  a.d[8] = {d_in[6], 512u * 1792u * 2u, F};
  a.d[9] = {d_in[7], 1792u * 512u * 2u, F};
  a.d[10] = {d_out, 2048u * 512u * 2u, F};
  a.d[11] = {d_ws, 0u, F};
  fused_block<<<256, 256, 0, stream>>>(a);
}
```
